```python
import torch

def _softmax(x, axis=-1):
    return torch.softmax(x, dim=axis)
def _gelu(x):
    return torch.nn.functional.gelu(x, approximate="tanh")
def _silu(x):
    return torch.nn.functional.silu(x)
def _sigmoid(x):
    return torch.sigmoid(x)
def _layernorm(x, axis=-1, eps=1e-5):
    m = x.mean(dim=axis, keepdim=True)
    v = x.var(dim=axis, keepdim=True, unbiased=False)
    return (x - m) / torch.sqrt(v + eps)
def _scatter_add(tgt, idx, src, axis=0):
    return tgt.clone().index_add_(axis, idx, src)
def _segment_sum(src, seg, n):
    out = torch.zeros((n,) + src.shape[1:], dtype=src.dtype)
    return out.index_add_(0, seg, src)
def _pad(x, pw):
    flat = tuple(v for pair in reversed(pw) for v in pair)
    return torch.nn.functional.pad(x, flat)

kernel_name = 'standalone__adm_resblock__diffusion'

def setup_inputs():

    _w = (lambda a: (lambda t: t.to(torch.bfloat16) if t.is_floating_point() else t)(torch.as_tensor(a)))
    B, C, HW, E = 2, 512, 32, 768
    sc = (9.0 * C) ** -0.5
    return {
        "x": _w((lambda *s: torch.randn(*s, dtype=torch.bfloat16) * 0.05)(B, C, HW, HW)),
        "t_emb": _w((lambda *s: torch.randn(*s, dtype=torch.bfloat16) * 0.4)(B, E)),
        "g1": _w(1.0 + 0.1 * (lambda *s: torch.randn(*s, dtype=torch.bfloat16) * 0.4)(C)),
        "be1": _w(0.1 * (lambda *s: torch.randn(*s, dtype=torch.bfloat16) * 0.4)(C)),
        "w1": _w((lambda *s: torch.randn(*s, dtype=torch.bfloat16) * 0.4)(C, C, 3, 3) * sc),
        "w_emb": _w((lambda *s: torch.randn(*s, dtype=torch.bfloat16) * 0.4)(E, C) * (E ** -0.5)),
        "g2": _w(1.0 + 0.1 * (lambda *s: torch.randn(*s, dtype=torch.bfloat16) * 0.4)(C)),
        "be2": _w(0.1 * (lambda *s: torch.randn(*s, dtype=torch.bfloat16) * 0.4)(C)),
        "w2": _w((lambda *s: torch.randn(*s, dtype=torch.bfloat16) * 0.4)(C, C, 3, 3) * sc),
    }

def reference(x, t_emb, g1, be1, w1, w_emb, g2, be2, w2):
    B, C, H, W = x.shape
    G, eps = 4, 1e-5
    def gn(z, gamma, beta):
        zr = z.reshape(B, G, (C // G) * H * W)
        mu = zr.mean(-1).reshape(B, G, 1)
        var = ((zr - mu) ** 2).mean(-1).reshape(B, G, 1)
        zn = ((zr - mu) / torch.sqrt(var + eps)).reshape(B, C, H, W)
        return zn * gamma.reshape(1, C, 1, 1) + beta.reshape(1, C, 1, 1)
    def conv3(z, w):
        zp = _pad(z, ((0, 0), (0, 0), (1, 1), (1, 1)))
        taps = [torch.einsum("bchw,oc->bohw", zp[:, :, i:i+H, j:j+W], w[:, :, i, j]) for i in range(3) for j in range(3)]
        out = taps[0]
        for t in taps[1:]:
            out = out + t
        return out
    h = conv3(_silu(gn(x, g1, be1)), w1)
    emb = _silu(t_emb) @ w_emb
    h = h + emb.reshape(B, C, 1, 1)
    h = conv3(_silu(gn(h, g2, be2)), w2)
    return x + h
```

```cpp
#include <hip/hip_runtime.h>
#include <hip/hip_bf16.h>
typedef unsigned short u16;
__device__ __forceinline__ float bf2f(u16 v){ return __uint_as_float(((unsigned)v)<<16); }
__device__ __forceinline__ u16 f2bf(float f){ __hip_bfloat16 b = __float2bfloat16(f); return *(u16*)&b; }

template<bool F32IN>
__global__ void stats_k(const void* in, float* st){
  int bg = blockIdx.x;
  const int N = 128*1024;
  double s=0, s2=0;
  for(int i=threadIdx.x;i<N;i+=blockDim.x){
    float v = F32IN ? ((const float*)in)[(size_t)bg*N+i] : bf2f(((const u16*)in)[(size_t)bg*N+i]);
    s+=v; s2+=(double)v*v;
  }
  __shared__ double sh[2][256];
  sh[0][threadIdx.x]=s; sh[1][threadIdx.x]=s2; __syncthreads();
  for(int o=128;o>0;o>>=1){ if(threadIdx.x<o){sh[0][threadIdx.x]+=sh[0][threadIdx.x+o]; sh[1][threadIdx.x]+=sh[1][threadIdx.x+o];} __syncthreads(); }
  if(threadIdx.x==0){ double m=sh[0][0]/N; double var=sh[1][0]/N-m*m; st[bg*2]=(float)m; st[bg*2+1]=(float)(1.0/sqrt(var+1e-5)); }
}
template<bool F32IN>
__global__ void apply_k(const void* in, const float* st, const u16* g, const u16* be, u16* out){
  int i = blockIdx.x*blockDim.x+threadIdx.x;
  int c = (i>>10)&511; int b = i>>19; int bg = b*4 + (c>>7);
  float v = F32IN ? ((const float*)in)[i] : bf2f(((const u16*)in)[i]);
  float z = (v-st[bg*2])*st[bg*2+1]*bf2f(g[c])+bf2f(be[c]);
  float y = z/(1.f+__expf(-z));
  out[i]=f2bf(y);
}
__global__ void emb_k(const u16* t, const u16* w, float* emb){
  int i = blockIdx.x*blockDim.x+threadIdx.x;
  int b=i>>9, c=i&511; float s=0;
  for(int e=0;e<768;e++){ float z=bf2f(t[b*768+e]); float y=z/(1.f+__expf(-z)); y=bf2f(f2bf(y)); s+=y*bf2f(w[e*512+c]); }
  emb[i]=s;
}
template<bool FIRST>
__global__ void conv_k(const u16* a, const u16* w, const float* emb, const u16* x, float* hout, u16* out){
  int i = blockIdx.x*blockDim.x+threadIdx.x;
  int px=i&31, py=(i>>5)&31, o=(i>>10)&511, b=i>>19;
  float s=0;
  for(int c=0;c<512;c++){
    const u16* ap = a + ((size_t)(b*512+c))*1024;
    const u16* wp = w + ((size_t)o*512+c)*9;
    for(int dy=0;dy<3;dy++){ int yy=py+dy-1; if(yy<0||yy>31) continue;
      for(int dx=0;dx<3;dx++){ int xx=px+dx-1; if(xx<0||xx>31) continue;
        s += bf2f(ap[yy*32+xx])*bf2f(wp[dy*3+dx]); } }
  }
  if(FIRST) hout[i]=s+emb[b*512+o]; else out[i]=f2bf(s+bf2f(x[i]));
}
extern "C" void kernel_launch(void* const* d_in, const int* in_sizes, int n_in,
                              void* d_out, int out_size, void* d_ws, size_t ws_size,
                              hipStream_t stream) {
  const u16* x=(const u16*)d_in[0]; const u16* t=(const u16*)d_in[1];
  const u16* g1=(const u16*)d_in[2]; const u16* be1=(const u16*)d_in[3];
  const u16* w1=(const u16*)d_in[4]; const u16* we=(const u16*)d_in[5];
  const u16* g2=(const u16*)d_in[6]; const u16* be2=(const u16*)d_in[7];
  const u16* w2=(const u16*)d_in[8];
  char* ws=(char*)d_ws;
  float* st=(float*)ws; float* emb=(float*)(ws+4096); u16* a=(u16*)(ws+65536); float* h=(float*)(ws+65536+4*1024*1024);
  int N=2*512*1024;
  stats_k<false><<<8,256,0,stream>>>(x,st);
  apply_k<false><<<N/256,256,0,stream>>>(x,st,g1,be1,a);
  emb_k<<<4,256,0,stream>>>(t,we,emb);
  conv_k<true><<<N/256,256,0,stream>>>(a,w1,emb,x,h,(u16*)d_out);
  stats_k<true><<<8,256,0,stream>>>(h,st+64);
  apply_k<true><<<N/256,256,0,stream>>>(h,st+64,g2,be2,a);
  conv_k<false><<<N/256,256,0,stream>>>(a,w2,emb,x,h,(u16*)d_out);
}
```

```cpp
#include <hip/hip_runtime.h>
#include <hip/hip_bf16.h>

typedef unsigned short u16;
typedef unsigned int u32;
typedef unsigned long long u64;
typedef __attribute__((ext_vector_type(4))) unsigned u32x4;
typedef __attribute__((ext_vector_type(2))) unsigned u32x2;
typedef __attribute__((ext_vector_type(4))) float f32x4;
typedef __attribute__((ext_vector_type(2))) float f32x2;
typedef __attribute__((ext_vector_type(8))) __bf16 bf16x8;
typedef __attribute__((ext_vector_type(2))) __bf16 bf16x2;
#define GAS __attribute__((address_space(1)))
#define LAS __attribute__((address_space(3)))
#define DI __device__ __forceinline__
#define RLX_AGENT __ATOMIC_RELAXED, __HIP_MEMORY_SCOPE_AGENT

DI float bflo(u32 v) { return __uint_as_float(v << 16); }
DI float bfhi(u32 v) { return __uint_as_float(v & 0xffff0000u); }
DI u32 cvtpk(float lo, float hi) { f32x2 v = {lo, hi}; bf16x2 b = __builtin_convertvector(v, bf16x2); return __builtin_bit_cast(u32, b); }
DI u32 rfl(u32 x) { return __builtin_amdgcn_readfirstlane(x); }
DI float silu(float z) { return z * __builtin_amdgcn_rcpf(1.f + __expf(-z)); }
template <int CTRL> DI float dpp_mov(float v) {
  return __builtin_bit_cast(float, __builtin_amdgcn_update_dpp(0, __builtin_bit_cast(int, v), CTRL, 0xf, 0xf, true));
}
DI float rdl(float v, int l) { return __builtin_bit_cast(float, __builtin_amdgcn_readlane(__builtin_bit_cast(int, v), l)); }
#define ROW_SUM2(a, b) asm volatile("s_nop 1\n\t" \
    "v_add_f32_dpp %0, %0, %0 quad_perm:[1,0,3,2] row_mask:0xf bank_mask:0xf\n\tv_add_f32_dpp %1, %1, %1 quad_perm:[1,0,3,2] row_mask:0xf bank_mask:0xf\n\ts_nop 0\n\t" \
    "v_add_f32_dpp %0, %0, %0 quad_perm:[2,3,0,1] row_mask:0xf bank_mask:0xf\n\tv_add_f32_dpp %1, %1, %1 quad_perm:[2,3,0,1] row_mask:0xf bank_mask:0xf\n\ts_nop 0\n\t" \
    "v_add_f32_dpp %0, %0, %0 row_half_mirror row_mask:0xf bank_mask:0xf\n\tv_add_f32_dpp %1, %1, %1 row_half_mirror row_mask:0xf bank_mask:0xf\n\ts_nop 0\n\t" \
    "v_add_f32_dpp %0, %0, %0 row_mirror row_mask:0xf bank_mask:0xf\n\tv_add_f32_dpp %1, %1, %1 row_mirror row_mask:0xf bank_mask:0xf\n\ts_nop 0" : "+v"(a), "+v"(b))

constexpr u32 WS_EPOCH = 0;
constexpr u32 WS_GRAN  = 4096;
constexpr u32 WS_HINT  = WS_GRAN + 65536;
constexpr u32 WS_FLAG  = WS_HINT + 262144;
constexpr u32 WS_EMB   = WS_FLAG + 262144;
constexpr u32 WS_CTL_BYTES = 1048576;
constexpr u32 WS_IMG   = WS_CTL_BYTES;
constexpr u32 IMG_BYTES = 2097152;
constexpr u32 W_BYTES = 4718592;
#define NB_PRE 2
#define POLL_FEW 4
#define POLL_LONG 6
#define POLL_SHORT 1
constexpr u32 SPIN_CAP = 1u << 18;
constexpr u32 CODE_KB = 20;

constexpr int LDS_RING = 36864;
constexpr int LDS_MISC = 4 * LDS_RING;
constexpr int LDS_PAR  = LDS_MISC;
constexpr int LDS_EMB  = LDS_MISC + 1024;
constexpr int LDS_RED  = LDS_MISC + 3072;
constexpr int LDS_BYTES = LDS_MISC + 3216;

#define SB __builtin_amdgcn_sched_barrier(0)
#define SETM0(base, imm) asm volatile("s_add_u32 m0, %0, %1\n\ts_nop 0" :: "s"(base), "n"(imm) : "memory", "scc")
#define DMA(srd, voff, soff, imm) asm volatile("buffer_load_dwordx4 %0, %1, %2 offen offset:" #imm " lds" :: "v"(voff), "s"(srd), "s"(soff) : "memory")
#define ALOAD(dst, voff, soff, imm) asm volatile("buffer_load_dwordx4 %0, %1, %2, %3 offen offset:" #imm " sc1" : "=v"(dst) : "v"(voff), "s"(srdA), "s"(soff) : "memory")
#define ELOAD() asm volatile("buffer_load_dwordx4 %0, %1, %2, 0 offen sc1" : "=a"(EG) : "v"(vegc), "s"(srdC) : "memory")
#define LDSR(base, off) (*(const LAS u32x4*)((base) + (off)))
#define PERM_E(hi, lo) __builtin_amdgcn_perm(hi, lo, 0x07060100u)
#define PERM_O(hi, lo) __builtin_amdgcn_alignbit(hi, lo, 16)
#define SHR1(x) (u32)__builtin_amdgcn_update_dpp(0, (int)(x), 0x111, 0xf, 0xf, true)
#define SHL1(x) (u32)__builtin_amdgcn_update_dpp(0, (int)(x), 0x101, 0xf, 0xf, true)
DI bf16x8 frag(const u32x4& v) { return __builtin_bit_cast(bf16x8, v); }
DI bf16x8 frag(const u32 (&a)[4]) { const u32x4 v = {a[0], a[1], a[2], a[3]}; return __builtin_bit_cast(bf16x8, v); }
#define MF(c, w, a) c = __builtin_amdgcn_mfma_f32_16x16x32_bf16(frag(w), frag(a), c, 0, 0, 0)
#define MF0(c, w, a) c = __builtin_amdgcn_mfma_f32_16x16x32_bf16(frag(w), frag(a), (f32x4){0.f, 0.f, 0.f, 0.f}, 0, 0, 0)
#define PIN9(R) asm volatile("" : "+v"(R[0]), "+v"(R[1]), "+v"(R[2]), "+v"(R[3]), "+v"(R[4]), "+v"(R[5]), "+v"(R[6]), "+v"(R[7]), "+v"(R[8]) :: "memory")
#define MAX_TRIES 24u
#define CHKR(dst, c) asm volatile("v_accvgpr_read_b32 %0, %1" : "=v"(dst) : "a"((c)[0]))
#define FLAGST() asm volatile("buffer_store_dword %0, %1, %2, %3 offen sc1" :: "v"(vtag), "v"(vl0), "s"(srdC), "s"(sflag) : "memory")
#define POISON(imm) asm volatile("buffer_store_dwordx4 %0, %1, %2, 0 offen offset:" #imm " sc1\n\ts_nop 1" :: "a"(poison), "v"(dbase), "s"(srdPn) : "memory")
#define CHK(final_) do { \
    cs_ = (ck0 + ck1) + (ck2 + ck3); \
    fail = (final_) + 1; asm volatile("" : "+s"(fail)); \
    if (__builtin_expect((((final_) ? ebad : 0ull) | __ballot(cs_ != cs_)) & chkmask, 0)) goto retry_; } while (0)
#define HINT() __builtin_amdgcn_raw_buffer_store_b32(tag, srdC, vh0, shint + cv * 131072u, 16)
#define DMA_HALFK(k) do { const u32 m0k_ = (((k) & 1u) ? m0h1 : m0h0) + ((k) >> 1) * 18432u, sok_ = SWV(0, (k) >> 1, (k) & 1u); DMA_HALF(srdW, m0k_, 0, sok_); } while (0)
#define REDD(j, e, ob, k) asm volatile("ds_write_b32 %0, %1 offset:%2" :: "v"(redw), "a"(C[j][e][ob][k]), "n"((((j) * 2 + (e)) * 2 + (ob)) * 1024 + ((k) >> 1) * 512 + ((k) & 1) * 4) : "memory")
#define SETM0N(base, imm) asm volatile("s_add_u32 m0, %0, %1" :: "s"(base), "n"(imm) : "memory", "scc")
#define RDP(t, src) RD64(P[t][(src) - 1], rb##src, ((t) >> 1) * 2048 + ((t) & 1) * 512)
#define WT3(n, t) asm volatile("s_waitcnt lgkmcnt(" #n ")" : "+v"(P[t][0]), "+v"(P[t][1]), "+v"(P[t][2]) :: "memory")
#define FIN(t) Hn[((t) >> 1) & 1][(t) >> 2] += (f32x4){P[t][0][0], P[t][0][1], P[(t) + 1][0][0], P[(t) + 1][0][1]}
#define ADD2(a, b) do { float a0_ = (a)[0], a1_ = (a)[1]; asm volatile("v_add_f32 %0, %0, %2\n\tv_add_f32 %1, %1, %3" : "+v"(a0_), "+v"(a1_) : "v"((b)[0]), "v"((b)[1])); (a) = (f32x2){a0_, a1_}; } while (0)
#define ACCMV(dst, c) asm volatile("v_accvgpr_read_b32 %0, %1" : "=v"(dst) : "a"(c))
#define ADD1(dst, e) asm volatile("v_add_f32 %0, %0, %1" : "+v"(dst) : "v"(e))
#define MFV(c, w, a) asm volatile("v_mfma_f32_16x16x32_bf16 %0, %1, %2, %0" : "+v"(c) : "v"(frag(w)), "v"(frag(a)))
#define EMB0() do { asm volatile("v_accvgpr_read_b32 %0, %2\n\tv_accvgpr_read_b32 %1, %3" : "=v"(e0), "=v"(e1) : "a"(EG[0]), "a"(EG[2])); } while (0)
#define EMB1() do { asm volatile("v_accvgpr_read_b32 %0, %2\n\tv_accvgpr_read_b32 %1, %3" : "=v"(et0), "=v"(et1) : "a"(EG[1]), "a"(EG[3])); } while (0)
#define EMB2() do { e0 += dpp_mov<0xB1>(e0); e1 += dpp_mov<0xB1>(e1); asm volatile("" : "+v"(e0), "+v"(e1)); } while (0)
#define EMB3() do { e0 += dpp_mov<0x4E>(e0); e1 += dpp_mov<0x4E>(e1); asm volatile("" : "+v"(e0), "+v"(e1)); } while (0)
#define EMB4() do { e0 += dpp_mov<0x141>(e0); e1 += dpp_mov<0x141>(e1); asm volatile("" : "+v"(e0), "+v"(e1)); } while (0)
#define EMB5() do { f0 = dpp_mov<0x128>(e0); f1 = dpp_mov<0x128>(e1); asm volatile("" : "+v"(f0), "+v"(f1)); } while (0)
#define EMB6() do { ev[0] = r < 8 ? e0 : f0; ev[1] = r < 8 ? e1 : f1; asm volatile("" : "+v"(ev[0]), "+v"(ev[1])); } while (0)
#define EMB7() do { ev[2] = r < 8 ? f0 : e0; ev[3] = r < 8 ? f1 : e1; asm volatile("" : "+v"(ev[2]), "+v"(ev[3])); } while (0)
#define EMB8() do { ebad = __ballot(et0 != etag || et1 != etag); asm volatile("" : "+s"(ebad)); } while (0)
#define RD64(dst, base, off) asm volatile("ds_read_b64 %0, %1 offset:%2" : "=v"(dst) : "v"(base), "n"(off) : "memory")
#define BARRIER() do { asm volatile("s_waitcnt lgkmcnt(0)" ::: "memory"); __builtin_amdgcn_s_barrier(); asm volatile("" ::: "memory"); } while (0)
#define DMA_HALF(srd, m0v, m0i, soff) do { \
    SETM0(m0v, m0i);        DMA(srd, vw[0], soff, 0); DMA(srd, vw[1], soff, 1024); DMA(srd, vw[2], soff, 2048); DMA(srd, vw[3], soff, 3072); \
    SETM0(m0v, (m0i) + 4096); DMA(srd, vw[4], soff, 0); DMA(srd, vw[5], soff, 1024); DMA(srd, vw[6], soff, 2048); DMA(srd, vw[7], soff, 3072); \
    SETM0(m0v, (m0i) + 8192); DMA(srd, vw[8], soff, 0); } while (0)


__global__ __launch_bounds__(256, 1) void resblock_k(const u16* __restrict__ x, const u16* __restrict__ temb,
    const u16* __restrict__ g1, const u16* __restrict__ be1, const u16* __restrict__ w1, const u16* __restrict__ wemb,
    const u16* __restrict__ g2, const u16* __restrict__ be2, const u16* __restrict__ w2, char* __restrict__ ws, u16* __restrict__ out
    ) {
  __shared__ __attribute__((aligned(16))) char lds[LDS_BYTES];
  const u64 pc0 = __builtin_amdgcn_s_getpc();
  const u32 tid = threadIdx.x, lane = tid & 63;
  {
    const u32 co = (tid < CODE_KB * 8u ? tid : 0u) * 128u;
    asm volatile("global_load_dword a252, %0, %1" :: "v"(co), "s"(pc0) : "memory", "a252");
  }
  asm volatile("" : "+s"(w2), "+s"(out), "+s"(ws), "+s"(w1));
  const u32 w = rfl(tid >> 6);
  const u32 bid = blockIdx.x, grp = bid & 7, idx = bid >> 3, b = grp >> 2, g = grp & 3;
  const u32 oi = idx & 3, rt = idx >> 2, ot = 4 * g + oi, y0 = 4 * rt, o0 = 32 * ot;
  const u32 rotO = rt & 1, rotH = (rt >> 1) & 1;
  const u32 q = lane >> 4, r = lane & 15u, cq = q ^ (q >> 1);
  const u32 rotW = rotO ^ (w & 1u), wp = w >> 1;
  const u32 obo = rotW ^ 1u, yo = y0 + 2u * wp;
  const u32 xoff = ((b * 512u + o0 + 16u * obo + 4u * cq) * 1024u + yo * 32u + 2u * r) * 2u;
  {
    const __amdgpu_buffer_rsrc_t srdX = __builtin_amdgcn_make_buffer_rsrc((void*)x, (short)0, IMG_BYTES, 0x00020000);
    const u32 s4k = 4096u;
    asm volatile("buffer_load_dword a224, %0, %1, 0 offen\n\tbuffer_load_dword a228, %0, %1, 0 offen offset:64\n\t"
                 "buffer_load_dword a225, %0, %1, 0 offen offset:2048\n\tbuffer_load_dword a229, %0, %1, 0 offen offset:2112\n\t"
                 "buffer_load_dword a226, %0, %1, %2 offen\n\tbuffer_load_dword a230, %0, %1, %2 offen offset:64\n\t"
                 "buffer_load_dword a227, %0, %1, %2 offen offset:2048\n\tbuffer_load_dword a231, %0, %1, %2 offen offset:2112"
                 :: "v"(xoff), "s"(srdX), "s"(s4k) : "memory", "a224", "a225", "a226", "a227", "a228", "a229", "a230", "a231");
  }
  asm volatile("global_load_dword a232, %0, %1 sc1" :: "v"(WS_EPOCH + b * 256u), "s"(ws) : "memory", "a232");
  {
    const u16* pp = ((q == 0) ? g1 : (q == 1) ? be1 : (q == 2) ? g2 : be2) + o0 + 2u * r;
    asm volatile("global_load_dword a233, %0, off" :: "v"(pp) : "memory", "a233");
  }
  const u32 dbase = (((b * 8u + (ot >> 1)) * 2u + (q >> 1)) * 32u + yo) * 2048u + (q & 1u) * 1024u + ((ot & 1u) * 2u + obo) * 256u + r * 16u;
  const u32x4 poison = {0xffffffffu, 0xffffffffu, 0xffffffffu, 0xffffffffu};
  {
    const __amdgpu_buffer_rsrc_t srdI = __builtin_amdgcn_make_buffer_rsrc((void*)(ws + WS_IMG), (short)0, IMG_BYTES, 0x00020000);
    __builtin_amdgcn_raw_buffer_store_b128(poison, srdI, dbase, 0, 16);
    __builtin_amdgcn_raw_buffer_store_b128(poison, srdI, dbase + 2048u, 0, 16);
    SB;
  }
  u32 vw[9];
  {
    const u32 l16 = lane * 16u;
    const u32 xf[4] = {l16, l16 ^ 16u, l16 ^ 64u, l16 ^ 80u};
#pragma unroll
    for (int i = 0; i < 9; i++) {
      const int r0 = (8 * i) / 9, r1 = r0 + 1 > 7 ? 7 : r0 + 1, lb = (9 * (r0 + 1) - 8 * i) * 8;
      const int g0 = (r0 & 3) | ((r0 & 4) << 1), g1_ = (r1 & 3) | ((r1 & 4) << 1);
      const u32 c0 = (u32)(g0 * 9216 + (8 * i - 9 * r0) * 128 - 1024 * (i & 3)), c1 = (u32)(g1_ * 9216 + (8 * i - 9 * r1) * 128 - 1024 * (i & 3));
      const u32 v0 = xf[(r0 >> 1) & 3] + c0, v1 = xf[(r1 >> 1) & 3] + c1;
      vw[i] = (lb >= 64) ? v0 : (lane < (u32)lb ? v0 : v1);
    }
  }
  const u32 ldsb = (u32)(size_t)(LAS char*)lds;
  const u32 m0h0 = ldsb + w * LDS_RING + rotH * 9216u, m0h1 = ldsb + w * LDS_RING + (rotH ^ 1u) * 9216u;
  const u32 swb = o0 * 9216u + w * 2304u;
  #define SWV(S, ob, h) (swb + (S) * 1152u + ((ob) ^ rotW) * 147456u + ((h) ^ rotH) * 36864u)
  const u32 sw_0_0_0 = SWV(0, 0, 0), sw_0_0_1 = SWV(0, 0, 1);
  const u32 sw_1_0_0 = SWV(1, 0, 0), sw_1_0_1 = SWV(1, 0, 1), sw_1_1_0 = SWV(1, 1, 0), sw_1_1_1 = SWV(1, 1, 1);
  const __amdgpu_buffer_rsrc_t srdW0 = __builtin_amdgcn_make_buffer_rsrc((void*)w1, (short)0, W_BYTES, 0x00020000);
  const __amdgpu_buffer_rsrc_t srdC = __builtin_amdgcn_make_buffer_rsrc((void*)ws, (short)0, WS_CTL_BYTES, 0x00020000);
  const u32 gpub = WS_GRAN + (grp * 32u + idx) * 128u;
  DMA_HALF(srdW0, m0h0, 0, sw_0_0_0);
  u32 vl0 = lane == 0 ? 0u : 0x40000000u, lred = ldsb + LDS_RED + (w * 4u + q) * 8u, lrd = ldsb + LDS_RED + r * 8u;
  asm volatile("" : "+v"(vl0), "+v"(lred), "+v"(lrd));
#define XREAD(pre) asm volatile(pre "v_accvgpr_read_b32 %0, a224\n\tv_accvgpr_read_b32 %1, a225\n\tv_accvgpr_read_b32 %2, a226\n\tv_accvgpr_read_b32 %3, a227\n\t" \
                     "v_accvgpr_read_b32 %4, a228\n\tv_accvgpr_read_b32 %5, a229\n\tv_accvgpr_read_b32 %6, a230\n\tv_accvgpr_read_b32 %7, a231" \
                     : "=v"(xr[0][0]), "=v"(xr[0][1]), "=v"(xr[0][2]), "=v"(xr[0][3]), "=v"(xr[1][0]), "=v"(xr[1][1]), "=v"(xr[1][2]), "=v"(xr[1][3]) :: "memory")
  u32 tag;
  f32x4 H[2][2];
  {
    u32 xr[2][4], ep0;
    XREAD("s_waitcnt vmcnt(11)\n\t");
    asm volatile("v_accvgpr_read_b32 %0, a232" : "=v"(ep0));
    tag = rfl(ep0) * 0x9E3779B1u + 0x7F4A7C15u;
#pragma unroll
    for (int jj = 0; jj < 2; jj++) {
      H[0][jj] = (f32x4){bflo(xr[jj][0]), bflo(xr[jj][1]), bflo(xr[jj][2]), bflo(xr[jj][3])};
      H[1][jj] = (f32x4){bfhi(xr[jj][0]), bfhi(xr[jj][1]), bfhi(xr[jj][2]), bfhi(xr[jj][3])};
    }
  }
  auto stats_publish = [&](const int cv) {
    const f32x4 a4 = (H[0][0] + H[0][1]) + (H[1][0] + H[1][1]);
    const f32x4 q4 = (H[0][0] * H[0][0] + H[0][1] * H[0][1]) + (H[1][0] * H[1][0] + H[1][1] * H[1][1]);
    float s1 = (a4[0] + a4[1]) + (a4[2] + a4[3]), s2 = (q4[0] + q4[1]) + (q4[2] + q4[3]);
    ROW_SUM2(s1, s2);
    if (r == 0) { f32x2 t = {s1, s2}; *(LAS f32x2*)(LAS char*)(size_t)lred = t; }
    BARRIER();
    if (w == 0) {
      const f32x2 p = *(const LAS f32x2*)(LAS char*)(size_t)lrd;
      float t1 = p[0], t2 = p[1];
      ROW_SUM2(t1, t2);
      const u32x4 gr = {__float_as_uint(t1), tag, __float_as_uint(t2), tag};
      __builtin_amdgcn_raw_buffer_store_b128(gr, srdC, vl0, gpub + cv * 32768u, 16);
    }
    SB;
  };
  stats_publish(0);
  {
    const u32 e0 = 96u * rt + 32u * (w ? w - 1u : 0u) + (lane >> 3);
    const u32 wo = (e0 * 512u + o0 + 4u * (lane & 7u)) * 2u, to = (b * 768u + e0) * 2u;
    asm volatile("global_load_dwordx2 a[240:241], %0, %4\n\tglobal_load_dwordx2 a[242:243], %1, %4\n\tglobal_load_dwordx2 a[244:245], %2, %4\n\tglobal_load_dwordx2 a[246:247], %3, %4"
                 :: "v"(wo), "v"(wo + 8192u), "v"(wo + 16384u), "v"(wo + 24576u), "s"(wemb) : "memory", "a240", "a241", "a242", "a243", "a244", "a245", "a246", "a247");
    asm volatile("global_load_ushort a248, %0, %1\n\tglobal_load_ushort a249, %0, %1 offset:16\n\tglobal_load_ushort a250, %0, %1 offset:32\n\tglobal_load_ushort a251, %0, %1 offset:48"
                 :: "v"(to), "s"(temb) : "memory", "a248", "a249", "a250", "a251");
  }
  { u32 parv; asm volatile("v_accvgpr_read_b32 %0, a233" : "=v"(parv)); *(LAS u32*)((LAS char*)lds + LDS_PAR + tid * 4u) = parv; }
  u32 va[6];
  const LAS char* ra[8];
#pragma unroll
  for (int i = 0; i < 6; i++) { const int yy = (int)y0 - 1 + i; va[i] = (yy >= 0 && yy < 32) ? (u32)yy * 2048u + lane * 16u : 0x40000000u; }
  {
    const u32 rq = r >> 2;
    const u32 lq = (rq == 0) ? 0u : (rq == 3) ? 1u : (rq + 1u);
    const u32 rho = 4u * lq + (r & 3u);
    const u32 f = ((rho >> 1) & 1u) | (rho & 4u);
#pragma unroll
    for (int n = 0; n < 8; n++) {
      const u32 gp = 18u * q + n, pos = (gp & ~7u) | ((gp ^ f) & 7u);
      ra[n] = (const LAS char*)lds + w * LDS_RING + rho * 1152u + pos * 16u;
    }
  }
  u32 veg = WS_EMB + (((b * 16u + ot) * 8u + (r & 7u)) * 32u + 16u * obo + 4u * cq + 2u * (r >> 3)) * 8u;
  u32 go0 = lane < 32 ? lane * 128u : 0x40000000u, vh0 = lane < 4 ? lane * 16384u : 0x40000000u;
  u32 fo0; { const int prt = (int)rt - 1 + (int)(lane >> 2); fo0 = (lane < 12 && prt >= 0 && prt < 8) ? ((u32)prt * 4u + (lane & 3u)) * 128u : 0x40000000u; }
  u32 rb1 = ldsb + (w ^ 1u) * LDS_RING + 18432u + wp * 8192u + lane * 8u;
  u32 rb2 = ldsb + (w ^ 2u) * LDS_RING + 18432u + wp * 8192u + 1024u + lane * 8u;
  u32 rb3 = ldsb + (w ^ 3u) * LDS_RING + 18432u + wp * 8192u + lane * 8u;
  u32 redw = ldsb + w * LDS_RING + 18432u + lane * 8u;
  u32 lpar = ldsb + LDS_PAR + w * 256u + (16u * obo + 4u * cq) * 2u;
  u32 shint = WS_HINT + ((b * 16u + g) * 32u + idx) * 128u, spoll = WS_HINT + ((b * 4u + g) * 4u + w) * 4096u;
  u32 sflag0 = WS_FLAG + ((b * 16u + ot) * 32u + y0 + w) * 128u, sgath = WS_GRAN + grp * 4096u, sab = (b * 16u + 4u * w) * 65536u;
  asm volatile("" : "+v"(va[0]), "+v"(va[1]), "+v"(va[2]), "+v"(va[3]), "+v"(va[4]), "+v"(va[5]), "+v"(veg), "+v"(go0), "+v"(vh0), "+v"(fo0));
  asm volatile("" : "+v"(ra[0]), "+v"(ra[1]), "+v"(ra[2]), "+v"(ra[3]), "+v"(ra[4]), "+v"(ra[5]), "+v"(ra[6]), "+v"(ra[7]));
  asm volatile("" : "+v"(rb1), "+v"(rb2), "+v"(rb3), "+v"(redw), "+v"(lpar));
  asm volatile("" : "+s"(shint), "+s"(spoll), "+s"(sflag0), "+s"(sgath), "+s"(sab));
#pragma unroll 1
  for (int cv = 0; cv < 2; cv++) {
    const __amdgpu_buffer_rsrc_t srdW = __builtin_amdgcn_make_buffer_rsrc((void*)(cv ? w2 : w1), (short)0, W_BYTES, 0x00020000);
    const __amdgpu_buffer_rsrc_t srdWn = __builtin_amdgcn_make_buffer_rsrc((void*)w2, (short)0, cv ? 0 : W_BYTES, 0x00020000);
    const __amdgpu_buffer_rsrc_t srdA = __builtin_amdgcn_make_buffer_rsrc((void*)(ws + WS_IMG + cv * IMG_BYTES), (short)0, IMG_BYTES, 0x00020000);
    const __amdgpu_buffer_rsrc_t srdPn = __builtin_amdgcn_make_buffer_rsrc((void*)(ws + WS_IMG + IMG_BYTES), (short)0, cv ? 0 : IMG_BYTES, 0x00020000);
    const u32 sa0 = sab, sa1 = sab + 65536u, sa2 = sab + 131072u, sa3 = sab + 196608u;
    const float cl = -1.44269504089f;
    f32x4 gc, bc;
    {
      const LAS char* pp = (const LAS char*)(size_t)(lpar + cv * 128u);
      const u32x2 gv = *(const LAS u32x2*)pp, bv = *(const LAS u32x2*)(pp + 64);
      gc = (f32x4){bflo(gv[0]), bfhi(gv[0]), bflo(gv[1]), bfhi(gv[1])} * cl;
      bc = (f32x4){bflo(bv[0]), bfhi(bv[0]), bflo(bv[1]), bfhi(bv[1])} * cl;
      H[0][0] *= gc; H[0][1] *= gc; H[1][0] *= gc; H[1][1] *= gc;
      asm volatile("" : "+v"(H[0][0]), "+v"(H[0][1]), "+v"(H[1][0]), "+v"(H[1][1]), "+v"(bc));
    }
    u32 kb = cv ? 2u : 1u;
    if (w == 0) {
      const u32 gso = sgath + cv * 32768u;
      u32 go = go0;
      float u1 = 0.f, u2 = 0.f;
      for (u32 spins = 0;; ++spins) {
        const u32x4 gn = __builtin_amdgcn_raw_buffer_load_b128(srdC, go, gso, 16);
        if (gn[1] == tag && gn[3] == tag && go < 0x40000000u) { u1 = __uint_as_float(gn[0]); u2 = __uint_as_float(gn[2]); go = 0x40000000u; }
        const u64 miss = __ballot(go < 0x40000000u);
        if (miss == 0 || spins > SPIN_CAP) break;
        if (__popcll(miss) > POLL_FEW) __builtin_amdgcn_s_sleep(POLL_LONG); else __builtin_amdgcn_s_sleep(POLL_SHORT);
        asm volatile("" ::: "memory");
      }
      ROW_SUM2(u1, u2);
      const float t1 = rdl(u1, 0) + rdl(u1, 16), t2 = rdl(u2, 0) + rdl(u2, 16);
      const float mean_ = t1 * (1.f / 131072.f);
      const float var_ = fmaxf(t2 * (1.f / 131072.f) - mean_ * mean_, 0.f);
      const float rs_ = __builtin_amdgcn_rsqf(var_ + 1e-5f);
      const f32x2 mr = {-mean_ * rs_, rs_};
      if (lane == 0) *(LAS f32x2*)((LAS char*)lds + LDS_RED + 128) = mr;
      if (cv == 1 && g == 0 && idx == 0 && lane == 0) __hip_atomic_store((GAS u32*)(ws + WS_EPOCH + b * 256u), tag, RLX_AGENT);
    } else {
#pragma unroll 1
      for (u32 k_ = 0; k_ < NB_PRE; k_++, kb++) DMA_HALFK(kb);
      if (cv == 0) {
        u32 we[8], te[4];
        asm volatile("s_waitcnt vmcnt(%12)\n\t"
                     "v_accvgpr_read_b32 %0, a240\n\tv_accvgpr_read_b32 %1, a241\n\tv_accvgpr_read_b32 %2, a242\n\tv_accvgpr_read_b32 %3, a243\n\t"
                     "v_accvgpr_read_b32 %4, a244\n\tv_accvgpr_read_b32 %5, a245\n\tv_accvgpr_read_b32 %6, a246\n\tv_accvgpr_read_b32 %7, a247\n\t"
                     "v_accvgpr_read_b32 %8, a248\n\tv_accvgpr_read_b32 %9, a249\n\tv_accvgpr_read_b32 %10, a250\n\tv_accvgpr_read_b32 %11, a251"
                     : "=v"(we[0]), "=v"(we[1]), "=v"(we[2]), "=v"(we[3]), "=v"(we[4]), "=v"(we[5]), "=v"(we[6]), "=v"(we[7]), "=v"(te[0]), "=v"(te[1]), "=v"(te[2]), "=v"(te[3])
                     : "n"(9 * NB_PRE) : "memory");
        float ea[4] = {0.f, 0.f, 0.f, 0.f};
#pragma unroll
        for (int k = 0; k < 4; k++) {
          const float sv = silu(bflo(te[k]));
          ea[0] += sv * bflo(we[2 * k]); ea[1] += sv * bfhi(we[2 * k]); ea[2] += sv * bflo(we[2 * k + 1]); ea[3] += sv * bfhi(we[2 * k + 1]);
        }
#pragma unroll
        for (int k = 0; k < 4; k++) ea[k] += dpp_mov<0x128>(ea[k]);
        if (r < 8) { f32x4 t = {ea[0], ea[1], ea[2], ea[3]}; *(LAS f32x4*)((LAS char*)lds + LDS_EMB + (w * 4u + q - 4u) * 128u + r * 16u) = t; }
      }
    }
    BARRIER();
    {
      const f32x2 mr = *(const LAS f32x2*)((LAS char*)lds + LDS_RED + 128);
      if (w == 1) HINT();
      const f32x4 bb = gc * mr[0] + bc;
#pragma unroll
      for (int jj = 0; jj < 2; jj++) {
        u32 pk[2][2];
#pragma unroll
        for (int e = 0; e < 2; e++) {
          float z[4];
#pragma unroll
          for (int k = 0; k < 4; k++) {
            const float tt = __builtin_fmaf(H[e][jj][k], mr[1], bb[k]);
            z[k] = tt * __builtin_amdgcn_rcpf(__builtin_fmaf(__builtin_amdgcn_exp2f(tt), cl, cl));
          }
          pk[e][0] = cvtpk(z[0], z[1]); pk[e][1] = cvtpk(z[2], z[3]);
        }
        const auto s0 = __builtin_amdgcn_permlane16_swap(pk[0][0], pk[1][0], false, false);
        const auto s1_ = __builtin_amdgcn_permlane16_swap(pk[0][1], pk[1][1], false, false);
        const u32x4 o4 = {s0[0], s1_[0], s0[1], s1_[1]};
        __builtin_amdgcn_raw_buffer_store_b128(o4, srdA, dbase + jj * 2048u, 0, 16);
      }
    }
    SB;
#pragma unroll 1
    for (kb = rfl(kb); kb < 4u; kb++) DMA_HALFK(kb);
    if (cv == 0 && w == 2 && lane < 32) {
      const LAS float* ep = (const LAS float*)((LAS char*)lds + LDS_EMB) + lane;
      float es = 0.f;
#pragma unroll
      for (int k = 0; k < 12; k++) es += ep[32 * k];
      const u32x2 gr = {__float_as_uint(es), tag};
      __builtin_amdgcn_raw_buffer_store_b64(gr, srdC, lane * 8u, WS_EMB + ((b * 16u + ot) * 8u + rt) * 256u, 16);
    }
    {
      u32 fo = fo0;
      const u32 fso = spoll + cv * 131072u;
      for (u32 spins = 0;; ++spins) {
        const u32 fv = __builtin_amdgcn_raw_buffer_load_b32(srdC, fo, fso, 16);
        if (fv == tag) fo = 0x40000000u;
        const u64 miss = __ballot(fo < 0x40000000u);
        if (miss == 0 || spins > SPIN_CAP) break;
        if (__popcll(miss) > POLL_FEW) __builtin_amdgcn_s_sleep(POLL_LONG); else __builtin_amdgcn_s_sleep(POLL_SHORT);
        asm volatile("" ::: "memory");
      }
      asm volatile("s_waitcnt vmcnt(0)" ::: "memory");
    }
    auto wait_flags = [&]() {
      const int py = (int)y0 - 2 + (int)(lane >> 2);
      u32 fo = (lane < 32 && py >= 0 && py < 32) ? ((lane & 3u) * 32u + (u32)py) * 128u : 0x40000000u;
      const u32 fso = WS_FLAG + ((cv * 2u + b) * 16u + 4u * w) * 4096u;
      for (u32 spins = 0;; ++spins) {
        const u32 fv = __builtin_amdgcn_raw_buffer_load_b32(srdC, fo, fso, 16);
        if (fv == tag) fo = 0x40000000u;
        if (__ballot(fo < 0x40000000u) == 0 || spins > SPIN_CAP) break;
        __builtin_amdgcn_s_sleep(8);
        asm volatile("" ::: "memory");
      }
    };
    const u32 sflag = sflag0 + cv * 131072u;
    u32 vtag = tag;
    u64 chkmask = ~0ull;
    u32 tries = 0;
    u32x4 EG;
    f32x4 Hn[2][2];
    u32x4 A[2][6][2], R[2][9];
    u32 SL[6][4], SR[6][4], W[2][4], WF[9][4];
    f32x4 C[4][2][2];
    float ck0, ck1, ck2, ck3, cs_;
    f32x2 P[8][3];
    float e0, e1, f0, f1, ev[4], hv[2][2][4];
    u32 et0, et1;
    u64 ebad;
    const u32 etag = cv ? 0u : tag, vegc = cv ? 0x40000000u : veg;
    u32 fail = 0, wpa = wp, wpb = wp;
    asm volatile("" : "+s"(wpa)); asm volatile("" : "+s"(wpb));
    retry_:
    if (__builtin_expect(fail != 0, 0)) {
      asm volatile("s_waitcnt vmcnt(0)" ::: "memory");
      if (fail == 2) {
        tries = MAX_TRIES;
        _Pragma("unroll 1") for (u32 k_ = 0; k_ < 4u; k_++) DMA_HALFK(k_);
      }
      if (++tries > MAX_TRIES) {
        wait_flags();
        for (u32 spins = 0;; ++spins) {
          const u32x4 eg = __builtin_amdgcn_raw_buffer_load_b128(srdC, vegc, 0, 16);
          if (__all(eg[1] == etag && eg[3] == etag) || spins > SPIN_CAP) break;
          __builtin_amdgcn_s_sleep(8);
          asm volatile("" ::: "memory");
        }
        chkmask = 0;
      }
      asm volatile("s_waitcnt vmcnt(0)" ::: "memory");
    }
    {
    ALOAD(A[0][0][0], va[0], sa0, 0);
    ALOAD(A[0][0][1], va[0], sa0, 1024);
    ALOAD(A[0][1][0], va[1], sa0, 0);
    ALOAD(A[0][1][1], va[1], sa0, 1024);
    ALOAD(A[0][2][0], va[2], sa0, 0);
    ALOAD(A[0][2][1], va[2], sa0, 1024);
    ALOAD(A[0][3][0], va[3], sa0, 0);
    ALOAD(A[0][3][1], va[3], sa0, 1024);
    ALOAD(A[0][4][0], va[4], sa0, 0);
    ALOAD(A[0][4][1], va[4], sa0, 1024);
    ALOAD(A[0][5][0], va[5], sa0, 0);
    ALOAD(A[0][5][1], va[5], sa0, 1024);
    R[0][0] = LDSR(ra[0], 0);
    R[0][1] = LDSR(ra[1], 0);
    R[0][2] = LDSR(ra[2], 0);
    R[0][3] = LDSR(ra[3], 0);
    R[0][4] = LDSR(ra[4], 0);
    R[0][5] = LDSR(ra[5], 0);
    R[0][6] = LDSR(ra[6], 0);
    R[0][7] = LDSR(ra[7], 0);
    R[0][8] = LDSR(ra[0], 128);
    SB;
    WF[1][0] = PERM_O(R[0][1][1], R[0][0][0]);
    WF[1][1] = PERM_O(R[0][3][2], R[0][2][1]);
    WF[1][2] = PERM_O(R[0][5][3], R[0][4][2]);
    WF[1][3] = PERM_O(R[0][8][0], R[0][6][3]);
    WF[0][0] = PERM_E(R[0][1][0], R[0][0][0]);
    WF[0][1] = PERM_E(R[0][3][1], R[0][2][1]);
    WF[0][2] = PERM_E(R[0][5][2], R[0][4][2]);
    WF[0][3] = PERM_E(R[0][7][3], R[0][6][3]);
    WF[2][0] = PERM_E(R[0][1][1], R[0][0][1]);
    WF[2][1] = PERM_E(R[0][3][2], R[0][2][2]);
    WF[2][2] = PERM_E(R[0][5][3], R[0][4][3]);
    WF[2][3] = PERM_E(R[0][8][0], R[0][7][0]);
    WF[4][0] = PERM_E(R[0][1][2], R[0][0][2]);
    WF[4][1] = PERM_E(R[0][3][3], R[0][2][3]);
    WF[4][2] = PERM_E(R[0][6][0], R[0][5][0]);
    WF[4][3] = PERM_E(R[0][8][1], R[0][7][1]);
    WF[3][0] = PERM_O(R[0][1][2], R[0][0][1]);
    WF[3][1] = PERM_O(R[0][3][3], R[0][2][2]);
    WF[3][2] = PERM_O(R[0][6][0], R[0][4][3]);
    WF[3][3] = PERM_O(R[0][8][1], R[0][7][0]);
    WF[5][0] = PERM_O(R[0][1][3], R[0][0][2]);
    WF[5][1] = PERM_O(R[0][4][0], R[0][2][3]);
    WF[5][2] = PERM_O(R[0][6][1], R[0][5][0]);
    WF[5][3] = PERM_O(R[0][8][2], R[0][7][1]);
    WF[7][0] = PERM_O(R[0][2][0], R[0][0][3]);
    WF[7][1] = PERM_O(R[0][4][1], R[0][3][0]);
    WF[7][2] = PERM_O(R[0][6][2], R[0][5][1]);
    WF[7][3] = PERM_O(R[0][8][3], R[0][7][2]);
    WF[6][0] = PERM_E(R[0][1][3], R[0][0][3]);
    WF[6][1] = PERM_E(R[0][4][0], R[0][3][0]);
    WF[6][2] = PERM_E(R[0][6][1], R[0][5][1]);
    WF[6][3] = PERM_E(R[0][8][2], R[0][7][2]);
    WF[8][0] = PERM_E(R[0][2][0], R[0][1][0]);
    WF[8][1] = PERM_E(R[0][4][1], R[0][3][1]);
    WF[8][2] = PERM_E(R[0][6][2], R[0][5][2]);
    WF[8][3] = PERM_E(R[0][8][3], R[0][7][3]);
    SB;
    asm volatile("s_waitcnt vmcnt(11)" : "+v"(A[0][0][0]) :: "memory");
    MF0(C[0][0][0], WF[1], A[0][0][0]);
    SB;
    SR[0][0] = SHL1(A[0][0][0][0]);
    SR[0][1] = SHL1(A[0][0][0][1]);
    SB;
    MF0(C[0][1][0], WF[0], A[0][0][0]);
    SB;
    FLAGST();
    SB;
    asm volatile("s_waitcnt vmcnt(11)" : "+v"(A[0][0][1]) :: "memory");
    MF(C[0][1][0], WF[1], A[0][0][1]);
    SB;
    SR[0][2] = SHL1(A[0][0][0][2]);
    SR[0][3] = SHL1(A[0][0][0][3]);
    SB;
    MF(C[0][0][0], WF[2], A[0][0][1]);
    SB;
    SL[0][0] = SHR1(A[0][0][1][0]);
    SL[0][1] = SHR1(A[0][0][1][1]);
    SB;
    MF(C[0][1][0], WF[2], SR[0]);
    SB;
    SL[0][2] = SHR1(A[0][0][1][2]);
    SL[0][3] = SHR1(A[0][0][1][3]);
    SB;
    MF(C[0][0][0], WF[0], SL[0]);
    SB;
    asm volatile("s_waitcnt vmcnt(10)" : "+v"(A[0][1][0]) :: "memory");
    MF0(C[1][0][0], WF[1], A[0][1][0]);
    SB;
    SR[1][0] = SHL1(A[0][1][0][0]);
    SR[1][1] = SHL1(A[0][1][0][1]);
    SB;
    MF(C[0][0][0], WF[4], A[0][1][0]);
    SB;
    SR[1][2] = SHL1(A[0][1][0][2]);
    SR[1][3] = SHL1(A[0][1][0][3]);
    SB;
    MF0(C[1][1][0], WF[0], A[0][1][0]);
    SB;
    ALOAD(A[1][0][0], va[0], sa1, 0);
    SB;
    MF(C[0][1][0], WF[3], A[0][1][0]);
    SB;
    asm volatile("s_waitcnt vmcnt(10)" : "+v"(A[0][1][1]) :: "memory");
    MF(C[1][1][0], WF[1], A[0][1][1]);
    SB;
    SL[1][0] = SHR1(A[0][1][1][0]);
    SL[1][1] = SHR1(A[0][1][1][1]);
    SB;
    MF(C[0][1][0], WF[4], A[0][1][1]);
    SB;
    SL[1][2] = SHR1(A[0][1][1][2]);
    SL[1][3] = SHR1(A[0][1][1][3]);
    SB;
    MF(C[1][0][0], WF[2], A[0][1][1]);
    SB;
    ALOAD(A[1][0][1], va[0], sa1, 1024);
    SB;
    MF(C[0][0][0], WF[5], A[0][1][1]);
    SB;
    MF(C[1][1][0], WF[2], SR[1]);
    SB;
    MF(C[0][1][0], WF[5], SR[1]);
    SB;
    MF(C[1][0][0], WF[0], SL[1]);
    SB;
    ALOAD(A[1][1][0], va[1], sa1, 0);
    SB;
    MF(C[0][0][0], WF[3], SL[1]);
    SB;
    asm volatile("s_waitcnt vmcnt(11)" : "+v"(A[0][2][0]) :: "memory");
    MF0(C[2][0][0], WF[1], A[0][2][0]);
    SB;
    SR[2][0] = SHL1(A[0][2][0][0]);
    SR[2][1] = SHL1(A[0][2][0][1]);
    SB;
    MF(C[1][0][0], WF[4], A[0][2][0]);
    SB;
    SR[2][2] = SHL1(A[0][2][0][2]);
    SR[2][3] = SHL1(A[0][2][0][3]);
    SB;
    MF(C[0][0][0], WF[7], A[0][2][0]);
    SB;
    ALOAD(A[1][1][1], va[1], sa1, 1024);
    SB;
    MF0(C[2][1][0], WF[0], A[0][2][0]);
    SB;
    MF(C[1][1][0], WF[3], A[0][2][0]);
    SB;
    MF(C[0][1][0], WF[6], A[0][2][0]);
    SB;
    asm volatile("s_waitcnt vmcnt(11)" : "+v"(A[0][2][1]) :: "memory");
    MF(C[2][1][0], WF[1], A[0][2][1]);
    SB;
    ALOAD(A[1][2][0], va[2], sa1, 0);
    SB;
    MF(C[1][1][0], WF[4], A[0][2][1]);
    SB;
    SL[2][0] = SHR1(A[0][2][1][0]);
    SL[2][1] = SHR1(A[0][2][1][1]);
    SB;
    MF(C[0][1][0], WF[7], A[0][2][1]);
    SB;
    SL[2][2] = SHR1(A[0][2][1][2]);
    SL[2][3] = SHR1(A[0][2][1][3]);
    SB;
    MF(C[2][0][0], WF[2], A[0][2][1]);
    SB;
    MF(C[1][0][0], WF[5], A[0][2][1]);
    SB;
    ALOAD(A[1][2][1], va[2], sa1, 1024);
    SB;
    MF(C[0][0][0], WF[8], A[0][2][1]);
    SB;
    MF(C[2][1][0], WF[2], SR[2]);
    SB;
    MF(C[1][1][0], WF[5], SR[2]);
    SB;
    MF(C[0][1][0], WF[8], SR[2]);
    SB;
    ALOAD(A[1][3][0], va[3], sa1, 0);
    SB;
    MF(C[2][0][0], WF[0], SL[2]);
    SB;
    MF(C[1][0][0], WF[3], SL[2]);
    SB;
    MF(C[0][0][0], WF[6], SL[2]);
    SB;
    asm volatile("s_waitcnt vmcnt(13)" : "+v"(A[0][3][0]) :: "memory");
    MF0(C[3][0][0], WF[1], A[0][3][0]);
    SB;
    ALOAD(A[1][3][1], va[3], sa1, 1024);
    SB;
    MF(C[2][0][0], WF[4], A[0][3][0]);
    SB;
    SR[3][0] = SHL1(A[0][3][0][0]);
    SR[3][1] = SHL1(A[0][3][0][1]);
    SB;
    MF(C[1][0][0], WF[7], A[0][3][0]);
    SB;
    SR[3][2] = SHL1(A[0][3][0][2]);
    SR[3][3] = SHL1(A[0][3][0][3]);
    SB;
    MF0(C[3][1][0], WF[0], A[0][3][0]);
    SB;
    MF(C[2][1][0], WF[3], A[0][3][0]);
    SB;
    ALOAD(A[1][4][0], va[4], sa1, 0);
    SB;
    MF(C[1][1][0], WF[6], A[0][3][0]);
    SB;
    asm volatile("s_waitcnt vmcnt(14)" : "+v"(A[0][3][1]) :: "memory");
    MF(C[3][1][0], WF[1], A[0][3][1]);
    SB;
    SL[3][0] = SHR1(A[0][3][1][0]);
    SL[3][1] = SHR1(A[0][3][1][1]);
    SB;
    MF(C[2][1][0], WF[4], A[0][3][1]);
    SB;
    SL[3][2] = SHR1(A[0][3][1][2]);
    SL[3][3] = SHR1(A[0][3][1][3]);
    SB;
    MF(C[1][1][0], WF[7], A[0][3][1]);
    SB;
    ALOAD(A[1][4][1], va[4], sa1, 1024);
    SB;
    MF(C[3][0][0], WF[2], A[0][3][1]);
    SB;
    MF(C[2][0][0], WF[5], A[0][3][1]);
    SB;
    MF(C[1][0][0], WF[8], A[0][3][1]);
    SB;
    MF(C[3][1][0], WF[2], SR[3]);
    SB;
    ALOAD(A[1][5][0], va[5], sa1, 0);
    SB;
    MF(C[2][1][0], WF[5], SR[3]);
    SB;
    MF(C[1][1][0], WF[8], SR[3]);
    SB;
    R[1][0] = LDSR(ra[0], 18432);
    SB;
    MF(C[3][0][0], WF[0], SL[3]);
    SB;
    R[1][1] = LDSR(ra[1], 18432);
    SB;
    MF(C[2][0][0], WF[3], SL[3]);
    SB;
    R[1][2] = LDSR(ra[2], 18432);
    ALOAD(A[1][5][1], va[5], sa1, 1024);
    SB;
    MF(C[1][0][0], WF[6], SL[3]);
    SB;
    R[1][3] = LDSR(ra[3], 18432);
    SB;
    asm volatile("s_waitcnt vmcnt(16)" : "+v"(A[0][4][0]) :: "memory");
    MF(C[3][0][0], WF[4], A[0][4][0]);
    SB;
    R[1][4] = LDSR(ra[4], 18432);
    SR[4][0] = SHL1(A[0][4][0][0]);
    SB;
    MF(C[2][0][0], WF[7], A[0][4][0]);
    SB;
    R[1][5] = LDSR(ra[5], 18432);
    SR[4][1] = SHL1(A[0][4][0][1]);
    SB;
    MF(C[3][1][0], WF[3], A[0][4][0]);
    SB;
    R[1][6] = LDSR(ra[6], 18432);
    SR[4][2] = SHL1(A[0][4][0][2]);
    SB;
    MF(C[2][1][0], WF[6], A[0][4][0]);
    SB;
    R[1][7] = LDSR(ra[7], 18432);
    SR[4][3] = SHL1(A[0][4][0][3]);
    SB;
    asm volatile("s_waitcnt vmcnt(15)" : "+v"(A[0][4][1]) :: "memory");
    MF(C[3][1][0], WF[4], A[0][4][1]);
    SB;
    R[1][8] = LDSR(ra[0], 18560);
    SL[4][0] = SHR1(A[0][4][1][0]);
    SB;
    MF(C[2][1][0], WF[7], A[0][4][1]);
    SB;
    SL[4][1] = SHR1(A[0][4][1][1]);
    SL[4][2] = SHR1(A[0][4][1][2]);
    SB;
    MF(C[3][0][0], WF[5], A[0][4][1]);
    SB;
    SL[4][3] = SHR1(A[0][4][1][3]);
    SB;
    MF(C[2][0][0], WF[8], A[0][4][1]);
    SB;
    MF(C[3][1][0], WF[5], SR[4]);
    SB;
    MF(C[2][1][0], WF[8], SR[4]);
    SB;
    MF(C[3][0][0], WF[3], SL[4]);
    SB;
    W[1][0] = PERM_E(R[1][1][0], R[1][0][0]);
    W[1][1] = PERM_E(R[1][3][1], R[1][2][1]);
    SB;
    MF(C[2][0][0], WF[6], SL[4]);
    SB;
    W[1][2] = PERM_E(R[1][5][2], R[1][4][2]);
    W[1][3] = PERM_E(R[1][7][3], R[1][6][3]);
    SB;
    asm volatile("s_waitcnt vmcnt(14)" : "+v"(A[0][5][0]) :: "memory");
    MF(C[3][0][0], WF[7], A[0][5][0]);
    SB;
    SR[5][0] = SHL1(A[0][5][0][0]);
    SR[5][1] = SHL1(A[0][5][0][1]);
    SB;
    MF(C[3][1][0], WF[6], A[0][5][0]);
    SB;
    SR[5][2] = SHL1(A[0][5][0][2]);
    SR[5][3] = SHL1(A[0][5][0][3]);
    SB;
    asm volatile("s_waitcnt vmcnt(13)" : "+v"(A[0][5][1]) :: "memory");
    MF(C[3][1][0], WF[7], A[0][5][1]);
    SB;
    SL[5][0] = SHR1(A[0][5][1][0]);
    SL[5][1] = SHR1(A[0][5][1][1]);
    SB;
    MF(C[3][0][0], WF[8], A[0][5][1]);
    SB;
    SL[5][2] = SHR1(A[0][5][1][2]);
    SL[5][3] = SHR1(A[0][5][1][3]);
    SB;
    MF(C[3][1][0], WF[8], SR[5]);
    SB;
    MF(C[3][0][0], WF[6], SL[5]);
    SB;
    MF0(C[0][1][1], W[1], A[0][0][0]);
    SB;
    W[0][0] = PERM_O(R[1][1][1], R[1][0][0]);
    W[0][1] = PERM_O(R[1][3][2], R[1][2][1]);
    SB;
    MF0(C[1][1][1], W[1], A[0][1][0]);
    SB;
    W[0][2] = PERM_O(R[1][5][3], R[1][4][2]);
    W[0][3] = PERM_O(R[1][8][0], R[1][6][3]);
    SB;
    MF0(C[2][1][1], W[1], A[0][2][0]);
    SB;
    MF0(C[3][1][1], W[1], A[0][3][0]);
    SB;
    MF0(C[0][0][1], W[1], SL[0]);
    SB;
    R[0][0] = LDSR(ra[1], 128);
    CHKR(ck0, C[0][0][0]);
    SB;
    MF0(C[1][0][1], W[1], SL[1]);
    SB;
    R[0][1] = LDSR(ra[2], 128);
    CHKR(ck1, C[0][1][0]);
    SB;
    MF0(C[2][0][1], W[1], SL[2]);
    SB;
    R[0][2] = LDSR(ra[3], 128);
    CHKR(ck2, C[3][0][0]);
    SB;
    MF0(C[3][0][1], W[1], SL[3]);
    SB;
    R[0][3] = LDSR(ra[4], 128);
    CHKR(ck3, C[3][1][0]);
    SB;
    MF(C[0][0][1], W[0], A[0][0][0]);
    SB;
    R[0][4] = LDSR(ra[5], 128);
    W[1][0] = PERM_E(R[1][1][1], R[1][0][1]);
    SB;
    MF(C[1][0][1], W[0], A[0][1][0]);
    SB;
    R[0][5] = LDSR(ra[6], 128);
    W[1][1] = PERM_E(R[1][3][2], R[1][2][2]);
    SB;
    MF(C[2][0][1], W[0], A[0][2][0]);
    SB;
    R[0][6] = LDSR(ra[7], 128);
    W[1][2] = PERM_E(R[1][5][3], R[1][4][3]);
    SB;
    MF(C[3][0][1], W[0], A[0][3][0]);
    SB;
    R[0][7] = LDSR(ra[0], 256);
    W[1][3] = PERM_E(R[1][8][0], R[1][7][0]);
    SB;
    MF(C[0][1][1], W[0], A[0][0][1]);
    SB;
    R[0][8] = LDSR(ra[1], 256);
    SB;
    MF(C[1][1][1], W[0], A[0][1][1]);
    SB;
    CHK(0);
    SB;
    MF(C[2][1][1], W[0], A[0][2][1]);
    SB;
    MF(C[3][1][1], W[0], A[0][3][1]);
    SB;
    MF(C[0][0][1], W[1], A[0][0][1]);
    SB;
    W[0][0] = PERM_O(R[1][1][2], R[1][0][1]);
    W[0][1] = PERM_O(R[1][3][3], R[1][2][2]);
    SB;
    MF(C[1][0][1], W[1], A[0][1][1]);
    SB;
    W[0][2] = PERM_O(R[1][6][0], R[1][4][3]);
    W[0][3] = PERM_O(R[1][8][1], R[1][7][0]);
    SB;
    MF(C[2][0][1], W[1], A[0][2][1]);
    SB;
    MF(C[3][0][1], W[1], A[0][3][1]);
    SB;
    MF(C[0][1][1], W[1], SR[0]);
    SB;
    MF(C[1][1][1], W[1], SR[1]);
    SB;
    PIN9(R[0]);
    SETM0N(m0h0, 0);
    SB;
    MF(C[2][1][1], W[1], SR[2]);
    SB;
    DMA(srdW, vw[0], sw_1_0_0, 0);
    SB;
    MF(C[3][1][1], W[1], SR[3]);
    SB;
    MF(C[0][1][1], W[0], A[0][1][0]);
    SB;
    W[1][0] = PERM_E(R[1][1][2], R[1][0][2]);
    W[1][1] = PERM_E(R[1][3][3], R[1][2][3]);
    SB;
    MF(C[1][1][1], W[0], A[0][2][0]);
    SB;
    W[1][2] = PERM_E(R[1][6][0], R[1][5][0]);
    W[1][3] = PERM_E(R[1][8][1], R[1][7][1]);
    SB;
    MF(C[2][1][1], W[0], A[0][3][0]);
    SB;
    DMA(srdW, vw[1], sw_1_0_0, 1024);
    SB;
    MF(C[3][1][1], W[0], A[0][4][0]);
    SB;
    MF(C[0][0][1], W[0], SL[1]);
    SB;
    MF(C[1][0][1], W[0], SL[2]);
    SB;
    MF(C[2][0][1], W[0], SL[3]);
    SB;
    DMA(srdW, vw[2], sw_1_0_0, 2048);
    SB;
    MF(C[3][0][1], W[0], SL[4]);
    SB;
    MF(C[0][0][1], W[1], A[0][1][0]);
    SB;
    W[0][0] = PERM_O(R[1][1][3], R[1][0][2]);
    W[0][1] = PERM_O(R[1][4][0], R[1][2][3]);
    SB;
    MF(C[1][0][1], W[1], A[0][2][0]);
    SB;
    W[0][2] = PERM_O(R[1][6][1], R[1][5][0]);
    W[0][3] = PERM_O(R[1][8][2], R[1][7][1]);
    SB;
    MF(C[2][0][1], W[1], A[0][3][0]);
    SB;
    DMA(srdW, vw[3], sw_1_0_0, 3072);
    SB;
    MF(C[3][0][1], W[1], A[0][4][0]);
    SB;
    MF(C[0][1][1], W[1], A[0][1][1]);
    SB;
    MF(C[1][1][1], W[1], A[0][2][1]);
    SB;
    SETM0N(m0h0, 4096);
    SB;
    MF(C[2][1][1], W[1], A[0][3][1]);
    SB;
    DMA(srdW, vw[4], sw_1_0_0, 0);
    SB;
    MF(C[3][1][1], W[1], A[0][4][1]);
    SB;
    MF(C[0][0][1], W[0], A[0][1][1]);
    SB;
    W[1][0] = PERM_E(R[1][1][3], R[1][0][3]);
    W[1][1] = PERM_E(R[1][4][0], R[1][3][0]);
    SB;
    MF(C[1][0][1], W[0], A[0][2][1]);
    SB;
    W[1][2] = PERM_E(R[1][6][1], R[1][5][1]);
    W[1][3] = PERM_E(R[1][8][2], R[1][7][2]);
    SB;
    MF(C[2][0][1], W[0], A[0][3][1]);
    SB;
    DMA(srdW, vw[5], sw_1_0_0, 1024);
    SB;
    MF(C[3][0][1], W[0], A[0][4][1]);
    SB;
    MF(C[0][1][1], W[0], SR[1]);
    SB;
    MF(C[1][1][1], W[0], SR[2]);
    SB;
    MF(C[2][1][1], W[0], SR[3]);
    SB;
    DMA(srdW, vw[6], sw_1_0_0, 2048);
    SB;
    MF(C[3][1][1], W[0], SR[4]);
    SB;
    MF(C[0][1][1], W[1], A[0][2][0]);
    SB;
    W[0][0] = PERM_O(R[1][2][0], R[1][0][3]);
    W[0][1] = PERM_O(R[1][4][1], R[1][3][0]);
    SB;
    MF(C[1][1][1], W[1], A[0][3][0]);
    SB;
    W[0][2] = PERM_O(R[1][6][2], R[1][5][1]);
    W[0][3] = PERM_O(R[1][8][3], R[1][7][2]);
    SB;
    MF(C[2][1][1], W[1], A[0][4][0]);
    SB;
    DMA(srdW, vw[7], sw_1_0_0, 3072);
    SB;
    MF(C[3][1][1], W[1], A[0][5][0]);
    SB;
    MF(C[0][0][1], W[1], SL[2]);
    SB;
    MF(C[1][0][1], W[1], SL[3]);
    SB;
    SETM0N(m0h0, 8192);
    SB;
    MF(C[2][0][1], W[1], SL[4]);
    SB;
    DMA(srdW, vw[8], sw_1_0_0, 0);
    SB;
    MF(C[3][0][1], W[1], SL[5]);
    SB;
    MF(C[0][0][1], W[0], A[0][2][0]);
    SB;
    W[1][0] = PERM_E(R[1][2][0], R[1][1][0]);
    W[1][1] = PERM_E(R[1][4][1], R[1][3][1]);
    SB;
    MF(C[1][0][1], W[0], A[0][3][0]);
    SB;
    SETM0N(m0h1, 0);
    W[1][2] = PERM_E(R[1][6][2], R[1][5][2]);
    SB;
    MF(C[2][0][1], W[0], A[0][4][0]);
    SB;
    DMA(srdW, vw[0], sw_1_0_1, 0);
    SB;
    MF(C[3][0][1], W[0], A[0][5][0]);
    SB;
    W[1][3] = PERM_E(R[1][8][3], R[1][7][3]);
    SB;
    MF(C[0][1][1], W[0], A[0][2][1]);
    SB;
    MF(C[1][1][1], W[0], A[0][3][1]);
    SB;
    MF(C[2][1][1], W[0], A[0][4][1]);
    SB;
    DMA(srdW, vw[1], sw_1_0_1, 1024);
    SB;
    MF(C[3][1][1], W[0], A[0][5][1]);
    SB;
    MF(C[0][0][1], W[1], A[0][2][1]);
    SB;
    W[0][0] = PERM_O(R[0][5][3], R[0][4][2]);
    W[0][1] = PERM_O(R[0][8][0], R[0][6][3]);
    SB;
    MF(C[1][0][1], W[1], A[0][3][1]);
    SB;
    W[0][2] = PERM_O(R[0][1][1], R[0][0][0]);
    W[0][3] = PERM_O(R[0][3][2], R[0][2][1]);
    SB;
    MF(C[2][0][1], W[1], A[0][4][1]);
    SB;
    DMA(srdW, vw[2], sw_1_0_1, 2048);
    SB;
    MF(C[3][0][1], W[1], A[0][5][1]);
    SB;
    MF(C[0][1][1], W[1], SR[2]);
    SB;
    MF(C[1][1][1], W[1], SR[3]);
    SB;
    MF(C[2][1][1], W[1], SR[4]);
    SB;
    DMA(srdW, vw[3], sw_1_0_1, 3072);
    SB;
    MF(C[3][1][1], W[1], SR[5]);
    SB;
    asm volatile("s_waitcnt vmcnt(13)" : "+v"(A[1][0][0]), "+v"(A[1][0][1]), "+v"(A[1][1][0]), "+v"(A[1][1][1]), "+v"(A[1][2][0]), "+v"(A[1][2][1]), "+v"(A[1][3][0]), "+v"(A[1][3][1]), "+v"(A[1][4][0]), "+v"(A[1][4][1]), "+v"(A[1][5][0]), "+v"(A[1][5][1]) :: "memory");
    MF(C[0][0][0], W[0], A[1][0][0]);
    SB;
    W[1][0] = PERM_E(R[0][6][0], R[0][5][0]);
    W[1][1] = PERM_E(R[0][8][1], R[0][7][1]);
    SB;
    MF(C[1][0][0], W[0], A[1][1][0]);
    SB;
    SETM0N(m0h1, 4096);
    W[1][2] = PERM_E(R[0][1][2], R[0][0][2]);
    SB;
    MF(C[2][0][0], W[0], A[1][2][0]);
    SB;
    DMA(srdW, vw[4], sw_1_0_1, 0);
    SB;
    MF(C[3][0][0], W[0], A[1][3][0]);
    SB;
    W[1][3] = PERM_E(R[0][3][3], R[0][2][3]);
    SL[0][0] = SHR1(A[1][0][1][0]);
    SB;
    MF(C[0][1][0], W[0], A[1][0][1]);
    SB;
    R[1][0] = LDSR(ra[1], 18560);
    SL[0][1] = SHR1(A[1][0][1][1]);
    SB;
    MF(C[1][1][0], W[0], A[1][1][1]);
    SB;
    R[1][1] = LDSR(ra[2], 18560);
    SL[0][2] = SHR1(A[1][0][1][2]);
    SB;
    MF(C[2][1][0], W[0], A[1][2][1]);
    SB;
    R[1][2] = LDSR(ra[3], 18560);
    DMA(srdW, vw[5], sw_1_0_1, 1024);
    SB;
    MF(C[3][1][0], W[0], A[1][3][1]);
    SB;
    R[1][3] = LDSR(ra[4], 18560);
    SL[0][3] = SHR1(A[1][0][1][3]);
    SB;
    MF(C[0][0][0], W[1], A[1][1][0]);
    SB;
    R[1][4] = LDSR(ra[5], 18560);
    W[0][0] = PERM_O(R[0][6][2], R[0][5][1]);
    SB;
    MF(C[1][0][0], W[1], A[1][2][0]);
    SB;
    R[1][5] = LDSR(ra[6], 18560);
    W[0][1] = PERM_O(R[0][8][3], R[0][7][2]);
    SB;
    MF(C[2][0][0], W[1], A[1][3][0]);
    SB;
    R[1][6] = LDSR(ra[7], 18560);
    W[0][2] = PERM_O(R[0][2][0], R[0][0][3]);
    SB;
    MF(C[3][0][0], W[1], A[1][4][0]);
    SB;
    R[1][7] = LDSR(ra[0], 18688);
    W[0][3] = PERM_O(R[0][4][1], R[0][3][0]);
    SB;
    MF(C[0][1][0], W[1], A[1][1][1]);
    SB;
    R[1][8] = LDSR(ra[1], 18688);
    SL[1][0] = SHR1(A[1][1][1][0]);
    SB;
    MF(C[1][1][0], W[1], A[1][2][1]);
    SB;
    DMA(srdW, vw[6], sw_1_0_1, 2048);
    SB;
    MF(C[2][1][0], W[1], A[1][3][1]);
    SB;
    SL[1][1] = SHR1(A[1][1][1][1]);
    SL[1][2] = SHR1(A[1][1][1][2]);
    SB;
    MF(C[3][1][0], W[1], A[1][4][1]);
    SB;
    SL[1][3] = SHR1(A[1][1][1][3]);
    SL[2][0] = SHR1(A[1][2][1][0]);
    SB;
    MF(C[0][0][0], W[0], A[1][2][0]);
    SB;
    W[1][0] = PERM_E(R[0][5][2], R[0][4][2]);
    W[1][1] = PERM_E(R[0][7][3], R[0][6][3]);
    SB;
    MF(C[1][0][0], W[0], A[1][3][0]);
    SB;
    DMA(srdW, vw[7], sw_1_0_1, 3072);
    SB;
    MF(C[2][0][0], W[0], A[1][4][0]);
    SB;
    W[1][2] = PERM_E(R[0][1][0], R[0][0][0]);
    W[1][3] = PERM_E(R[0][3][1], R[0][2][1]);
    SB;
    MF(C[3][0][0], W[0], A[1][5][0]);
    SB;
    SL[2][1] = SHR1(A[1][2][1][1]);
    SL[2][2] = SHR1(A[1][2][1][2]);
    SB;
    MF(C[0][1][0], W[0], A[1][2][1]);
    SB;
    SETM0N(m0h1, 8192);
    SL[2][3] = SHR1(A[1][2][1][3]);
    SB;
    MF(C[1][1][0], W[0], A[1][3][1]);
    SB;
    PIN9(R[1]);
    DMA(srdW, vw[8], sw_1_0_1, 0);
    SB;
    MF(C[2][1][0], W[0], A[1][4][1]);
    SB;
    SL[3][0] = SHR1(A[1][3][1][0]);
    SL[3][1] = SHR1(A[1][3][1][1]);
    SB;
    MF(C[3][1][0], W[0], A[1][5][1]);
    SB;
    SL[3][2] = SHR1(A[1][3][1][2]);
    SL[3][3] = SHR1(A[1][3][1][3]);
    SB;
    MF(C[0][1][0], W[1], A[1][0][0]);
    SB;
    W[0][0] = PERM_O(R[0][6][0], R[0][4][3]);
    W[0][1] = PERM_O(R[0][8][1], R[0][7][0]);
    SB;
    MF(C[1][1][0], W[1], A[1][1][0]);
    SB;
    ALOAD(A[0][0][0], va[0], sa2, 0);
    SB;
    MF(C[2][1][0], W[1], A[1][2][0]);
    SB;
    W[0][2] = PERM_O(R[0][1][2], R[0][0][1]);
    W[0][3] = PERM_O(R[0][3][3], R[0][2][2]);
    SB;
    MF(C[3][1][0], W[1], A[1][3][0]);
    SB;
    SL[4][0] = SHR1(A[1][4][1][0]);
    SL[4][1] = SHR1(A[1][4][1][1]);
    SB;
    MF(C[0][0][0], W[1], SL[0]);
    SB;
    SL[4][2] = SHR1(A[1][4][1][2]);
    SL[4][3] = SHR1(A[1][4][1][3]);
    SB;
    MF(C[1][0][0], W[1], SL[1]);
    SB;
    ALOAD(A[0][0][1], va[0], sa2, 1024);
    SB;
    MF(C[2][0][0], W[1], SL[2]);
    SB;
    SL[5][0] = SHR1(A[1][5][1][0]);
    SL[5][1] = SHR1(A[1][5][1][1]);
    SB;
    MF(C[3][0][0], W[1], SL[3]);
    SB;
    SL[5][2] = SHR1(A[1][5][1][2]);
    SL[5][3] = SHR1(A[1][5][1][3]);
    SB;
    MF(C[0][1][0], W[0], A[1][1][0]);
    SB;
    W[1][0] = PERM_E(R[0][6][1], R[0][5][1]);
    W[1][1] = PERM_E(R[0][8][2], R[0][7][2]);
    SB;
    MF(C[1][1][0], W[0], A[1][2][0]);
    SB;
    ALOAD(A[0][1][0], va[1], sa2, 0);
    SB;
    MF(C[2][1][0], W[0], A[1][3][0]);
    SB;
    W[1][2] = PERM_E(R[0][1][3], R[0][0][3]);
    W[1][3] = PERM_E(R[0][4][0], R[0][3][0]);
    SB;
    MF(C[3][1][0], W[0], A[1][4][0]);
    SB;
    SR[0][0] = SHL1(A[1][0][0][0]);
    SR[0][1] = SHL1(A[1][0][0][1]);
    SB;
    MF(C[0][0][0], W[0], SL[1]);
    SB;
    SR[0][2] = SHL1(A[1][0][0][2]);
    SR[0][3] = SHL1(A[1][0][0][3]);
    SB;
    MF(C[1][0][0], W[0], SL[2]);
    SB;
    ALOAD(A[0][1][1], va[1], sa2, 1024);
    SB;
    MF(C[2][0][0], W[0], SL[3]);
    SB;
    SR[1][0] = SHL1(A[1][1][0][0]);
    SR[1][1] = SHL1(A[1][1][0][1]);
    SB;
    MF(C[3][0][0], W[0], SL[4]);
    SB;
    SR[1][2] = SHL1(A[1][1][0][2]);
    SR[1][3] = SHL1(A[1][1][0][3]);
    SB;
    MF(C[0][1][0], W[1], A[1][2][0]);
    SB;
    W[0][0] = PERM_E(R[0][5][3], R[0][4][3]);
    W[0][1] = PERM_E(R[0][8][0], R[0][7][0]);
    SB;
    MF(C[1][1][0], W[1], A[1][3][0]);
    SB;
    ALOAD(A[0][2][0], va[2], sa2, 0);
    SB;
    MF(C[2][1][0], W[1], A[1][4][0]);
    SB;
    W[0][2] = PERM_E(R[0][1][1], R[0][0][1]);
    W[0][3] = PERM_E(R[0][3][2], R[0][2][2]);
    SB;
    MF(C[3][1][0], W[1], A[1][5][0]);
    SB;
    SR[2][0] = SHL1(A[1][2][0][0]);
    SR[2][1] = SHL1(A[1][2][0][1]);
    SB;
    MF(C[0][0][0], W[1], SL[2]);
    SB;
    SR[2][2] = SHL1(A[1][2][0][2]);
    SR[2][3] = SHL1(A[1][2][0][3]);
    SB;
    MF(C[1][0][0], W[1], SL[3]);
    SB;
    ALOAD(A[0][2][1], va[2], sa2, 1024);
    SB;
    MF(C[2][0][0], W[1], SL[4]);
    SB;
    SR[3][0] = SHL1(A[1][3][0][0]);
    SR[3][1] = SHL1(A[1][3][0][1]);
    SB;
    MF(C[3][0][0], W[1], SL[5]);
    SB;
    SR[3][2] = SHL1(A[1][3][0][2]);
    SR[3][3] = SHL1(A[1][3][0][3]);
    SB;
    MF(C[0][0][0], W[0], A[1][0][1]);
    SB;
    W[1][0] = PERM_O(R[0][6][1], R[0][5][0]);
    W[1][1] = PERM_O(R[0][8][2], R[0][7][1]);
    SB;
    MF(C[1][0][0], W[0], A[1][1][1]);
    SB;
    ALOAD(A[0][3][0], va[3], sa2, 0);
    SB;
    MF(C[2][0][0], W[0], A[1][2][1]);
    SB;
    W[1][2] = PERM_O(R[0][1][3], R[0][0][2]);
    W[1][3] = PERM_O(R[0][4][0], R[0][2][3]);
    SB;
    MF(C[3][0][0], W[0], A[1][3][1]);
    SB;
    SR[4][0] = SHL1(A[1][4][0][0]);
    SR[4][1] = SHL1(A[1][4][0][1]);
    SB;
    MF(C[0][1][0], W[0], SR[0]);
    SB;
    SR[4][2] = SHL1(A[1][4][0][2]);
    SR[4][3] = SHL1(A[1][4][0][3]);
    SB;
    MF(C[1][1][0], W[0], SR[1]);
    SB;
    ALOAD(A[0][3][1], va[3], sa2, 1024);
    SB;
    MF(C[2][1][0], W[0], SR[2]);
    SB;
    SR[5][0] = SHL1(A[1][5][0][0]);
    SR[5][1] = SHL1(A[1][5][0][1]);
    SB;
    MF(C[3][1][0], W[0], SR[3]);
    SB;
    SR[5][2] = SHL1(A[1][5][0][2]);
    SR[5][3] = SHL1(A[1][5][0][3]);
    SB;
    MF(C[0][0][0], W[1], A[1][1][1]);
    SB;
    W[0][0] = PERM_E(R[0][6][2], R[0][5][2]);
    W[0][1] = PERM_E(R[0][8][3], R[0][7][3]);
    SB;
    MF(C[1][0][0], W[1], A[1][2][1]);
    SB;
    ALOAD(A[0][4][0], va[4], sa2, 0);
    SB;
    MF(C[2][0][0], W[1], A[1][3][1]);
    SB;
    W[0][2] = PERM_E(R[0][2][0], R[0][1][0]);
    W[0][3] = PERM_E(R[0][4][1], R[0][3][1]);
    SB;
    MF(C[3][0][0], W[1], A[1][4][1]);
    SB;
    MF(C[0][1][0], W[1], SR[1]);
    SB;
    MF(C[1][1][0], W[1], SR[2]);
    SB;
    ALOAD(A[0][4][1], va[4], sa2, 1024);
    SB;
    MF(C[2][1][0], W[1], SR[3]);
    SB;
    MF(C[3][1][0], W[1], SR[4]);
    SB;
    MF(C[0][0][0], W[0], A[1][2][1]);
    SB;
    W[1][0] = PERM_E(R[1][5][2], R[1][4][2]);
    W[1][1] = PERM_E(R[1][7][3], R[1][6][3]);
    SB;
    MF(C[1][0][0], W[0], A[1][3][1]);
    SB;
    ALOAD(A[0][5][0], va[5], sa2, 0);
    SB;
    MF(C[2][0][0], W[0], A[1][4][1]);
    SB;
    W[1][2] = PERM_E(R[1][1][0], R[1][0][0]);
    W[1][3] = PERM_E(R[1][3][1], R[1][2][1]);
    SB;
    MF(C[3][0][0], W[0], A[1][5][1]);
    SB;
    MF(C[0][1][0], W[0], SR[2]);
    SB;
    MF(C[1][1][0], W[0], SR[3]);
    SB;
    ALOAD(A[0][5][1], va[5], sa2, 1024);
    SB;
    MF(C[2][1][0], W[0], SR[4]);
    SB;
    MF(C[3][1][0], W[0], SR[5]);
    SB;
    MF(C[0][1][1], W[1], A[1][0][0]);
    SB;
    SETM0N(m0h0, 18432);
    W[0][0] = PERM_O(R[1][5][3], R[1][4][2]);
    SB;
    MF(C[1][1][1], W[1], A[1][1][0]);
    SB;
    DMA(srdW, vw[0], sw_1_1_0, 0);
    SB;
    MF(C[2][1][1], W[1], A[1][2][0]);
    SB;
    W[0][1] = PERM_O(R[1][8][0], R[1][6][3]);
    W[0][2] = PERM_O(R[1][1][1], R[1][0][0]);
    SB;
    MF(C[3][1][1], W[1], A[1][3][0]);
    SB;
    W[0][3] = PERM_O(R[1][3][2], R[1][2][1]);
    SB;
    MF(C[0][0][1], W[1], SL[0]);
    SB;
    MF(C[1][0][1], W[1], SL[1]);
    SB;
    DMA(srdW, vw[1], sw_1_1_0, 1024);
    SB;
    MF(C[2][0][1], W[1], SL[2]);
    SB;
    MF(C[3][0][1], W[1], SL[3]);
    SB;
    MF(C[0][0][1], W[0], A[1][0][0]);
    SB;
    W[1][0] = PERM_E(R[1][5][3], R[1][4][3]);
    W[1][1] = PERM_E(R[1][8][0], R[1][7][0]);
    SB;
    MF(C[1][0][1], W[0], A[1][1][0]);
    SB;
    DMA(srdW, vw[2], sw_1_1_0, 2048);
    SB;
    MF(C[2][0][1], W[0], A[1][2][0]);
    SB;
    W[1][2] = PERM_E(R[1][1][1], R[1][0][1]);
    W[1][3] = PERM_E(R[1][3][2], R[1][2][2]);
    SB;
    MF(C[3][0][1], W[0], A[1][3][0]);
    SB;
    MF(C[0][1][1], W[0], A[1][0][1]);
    SB;
    MF(C[1][1][1], W[0], A[1][1][1]);
    SB;
    DMA(srdW, vw[3], sw_1_1_0, 3072);
    SB;
    MF(C[2][1][1], W[0], A[1][2][1]);
    SB;
    MF(C[3][1][1], W[0], A[1][3][1]);
    SB;
    MF(C[0][0][1], W[1], A[1][0][1]);
    SB;
    SETM0N(m0h0, 22528);
    W[0][0] = PERM_O(R[1][6][0], R[1][4][3]);
    SB;
    MF(C[1][0][1], W[1], A[1][1][1]);
    SB;
    DMA(srdW, vw[4], sw_1_1_0, 0);
    SB;
    MF(C[2][0][1], W[1], A[1][2][1]);
    SB;
    W[0][1] = PERM_O(R[1][8][1], R[1][7][0]);
    W[0][2] = PERM_O(R[1][1][2], R[1][0][1]);
    SB;
    MF(C[3][0][1], W[1], A[1][3][1]);
    SB;
    W[0][3] = PERM_O(R[1][3][3], R[1][2][2]);
    SB;
    MF(C[0][1][1], W[1], SR[0]);
    SB;
    MF(C[1][1][1], W[1], SR[1]);
    SB;
    DMA(srdW, vw[5], sw_1_1_0, 1024);
    SB;
    MF(C[2][1][1], W[1], SR[2]);
    SB;
    MF(C[3][1][1], W[1], SR[3]);
    SB;
    MF(C[0][1][1], W[0], A[1][1][0]);
    SB;
    W[1][0] = PERM_E(R[1][6][0], R[1][5][0]);
    W[1][1] = PERM_E(R[1][8][1], R[1][7][1]);
    SB;
    MF(C[1][1][1], W[0], A[1][2][0]);
    SB;
    DMA(srdW, vw[6], sw_1_1_0, 2048);
    SB;
    MF(C[2][1][1], W[0], A[1][3][0]);
    SB;
    W[1][2] = PERM_E(R[1][1][2], R[1][0][2]);
    W[1][3] = PERM_E(R[1][3][3], R[1][2][3]);
    SB;
    MF(C[3][1][1], W[0], A[1][4][0]);
    SB;
    MF(C[0][0][1], W[0], SL[1]);
    SB;
    MF(C[1][0][1], W[0], SL[2]);
    SB;
    DMA(srdW, vw[7], sw_1_1_0, 3072);
    SB;
    MF(C[2][0][1], W[0], SL[3]);
    SB;
    MF(C[3][0][1], W[0], SL[4]);
    SB;
    MF(C[0][0][1], W[1], A[1][1][0]);
    SB;
    SETM0N(m0h0, 26624);
    W[0][0] = PERM_O(R[1][6][1], R[1][5][0]);
    SB;
    MF(C[1][0][1], W[1], A[1][2][0]);
    SB;
    DMA(srdW, vw[8], sw_1_1_0, 0);
    SB;
    MF(C[2][0][1], W[1], A[1][3][0]);
    SB;
    W[0][1] = PERM_O(R[1][8][2], R[1][7][1]);
    W[0][2] = PERM_O(R[1][1][3], R[1][0][2]);
    SB;
    MF(C[3][0][1], W[1], A[1][4][0]);
    SB;
    W[0][3] = PERM_O(R[1][4][0], R[1][2][3]);
    SB;
    MF(C[0][1][1], W[1], A[1][1][1]);
    SB;
    SETM0N(m0h1, 18432);
    SB;
    MF(C[1][1][1], W[1], A[1][2][1]);
    SB;
    DMA(srdW, vw[0], sw_1_1_1, 0);
    SB;
    MF(C[2][1][1], W[1], A[1][3][1]);
    SB;
    MF(C[3][1][1], W[1], A[1][4][1]);
    SB;
    MF(C[0][0][1], W[0], A[1][1][1]);
    SB;
    W[1][0] = PERM_E(R[1][6][1], R[1][5][1]);
    W[1][1] = PERM_E(R[1][8][2], R[1][7][2]);
    SB;
    MF(C[1][0][1], W[0], A[1][2][1]);
    SB;
    DMA(srdW, vw[1], sw_1_1_1, 1024);
    SB;
    MF(C[2][0][1], W[0], A[1][3][1]);
    SB;
    W[1][2] = PERM_E(R[1][1][3], R[1][0][3]);
    W[1][3] = PERM_E(R[1][4][0], R[1][3][0]);
    SB;
    MF(C[3][0][1], W[0], A[1][4][1]);
    SB;
    MF(C[0][1][1], W[0], SR[1]);
    SB;
    MF(C[1][1][1], W[0], SR[2]);
    SB;
    DMA(srdW, vw[2], sw_1_1_1, 2048);
    SB;
    MF(C[2][1][1], W[0], SR[3]);
    SB;
    MF(C[3][1][1], W[0], SR[4]);
    SB;
    MF(C[0][1][1], W[1], A[1][2][0]);
    SB;
    W[0][0] = PERM_O(R[1][6][2], R[1][5][1]);
    W[0][1] = PERM_O(R[1][8][3], R[1][7][2]);
    SB;
    MF(C[1][1][1], W[1], A[1][3][0]);
    SB;
    DMA(srdW, vw[3], sw_1_1_1, 3072);
    SB;
    MF(C[2][1][1], W[1], A[1][4][0]);
    SB;
    asm volatile("s_waitcnt vmcnt(25)" ::: "memory");
    R[0][0] = LDSR(ra[0], 0);
    W[0][2] = PERM_O(R[1][2][0], R[1][0][3]);
    SB;
    MF(C[3][1][1], W[1], A[1][5][0]);
    SB;
    R[0][1] = LDSR(ra[1], 0);
    W[0][3] = PERM_O(R[1][4][1], R[1][3][0]);
    SB;
    MF(C[0][0][1], W[1], SL[2]);
    SB;
    R[0][2] = LDSR(ra[2], 0);
    SETM0N(m0h1, 22528);
    SB;
    MF(C[1][0][1], W[1], SL[3]);
    SB;
    R[0][3] = LDSR(ra[3], 0);
    DMA(srdW, vw[4], sw_1_1_1, 0);
    SB;
    MF(C[2][0][1], W[1], SL[4]);
    SB;
    R[0][4] = LDSR(ra[4], 0);
    SB;
    MF(C[3][0][1], W[1], SL[5]);
    SB;
    R[0][5] = LDSR(ra[5], 0);
    SB;
    MF(C[0][0][1], W[0], A[1][2][0]);
    SB;
    R[0][6] = LDSR(ra[6], 0);
    W[1][0] = PERM_E(R[1][6][2], R[1][5][2]);
    SB;
    MF(C[1][0][1], W[0], A[1][3][0]);
    SB;
    R[0][7] = LDSR(ra[7], 0);
    W[1][1] = PERM_E(R[1][8][3], R[1][7][3]);
    SB;
    MF(C[2][0][1], W[0], A[1][4][0]);
    SB;
    R[0][8] = LDSR(ra[0], 128);
    W[1][2] = PERM_E(R[1][2][0], R[1][1][0]);
    SB;
    MF(C[3][0][1], W[0], A[1][5][0]);
    SB;
    DMA(srdW, vw[5], sw_1_1_1, 1024);
    SB;
    MF(C[0][1][1], W[0], A[1][2][1]);
    SB;
    W[1][3] = PERM_E(R[1][4][1], R[1][3][1]);
    SB;
    MF(C[1][1][1], W[0], A[1][3][1]);
    SB;
    MF(C[2][1][1], W[0], A[1][4][1]);
    SB;
    MF(C[3][1][1], W[0], A[1][5][1]);
    SB;
    DMA(srdW, vw[6], sw_1_1_1, 2048);
    SB;
    MF(C[0][0][1], W[1], A[1][2][1]);
    SB;
    W[0][0] = PERM_O(R[0][1][1], R[0][0][0]);
    W[0][1] = PERM_O(R[0][3][2], R[0][2][1]);
    SB;
    MF(C[1][0][1], W[1], A[1][3][1]);
    SB;
    W[0][2] = PERM_O(R[0][5][3], R[0][4][2]);
    W[0][3] = PERM_O(R[0][8][0], R[0][6][3]);
    SB;
    MF(C[2][0][1], W[1], A[1][4][1]);
    SB;
    MF(C[3][0][1], W[1], A[1][5][1]);
    SB;
    DMA(srdW, vw[7], sw_1_1_1, 3072);
    SB;
    MF(C[0][1][1], W[1], SR[2]);
    SB;
    MF(C[1][1][1], W[1], SR[3]);
    SB;
    MF(C[2][1][1], W[1], SR[4]);
    SB;
    SETM0N(m0h1, 26624);
    SB;
    MF(C[3][1][1], W[1], SR[5]);
    SB;
    DMA(srdW, vw[8], sw_1_1_1, 0);
    SB;
    asm volatile("s_waitcnt vmcnt(18)" : "+v"(A[0][0][0]), "+v"(A[0][0][1]), "+v"(A[0][1][0]), "+v"(A[0][1][1]), "+v"(A[0][2][0]), "+v"(A[0][2][1]), "+v"(A[0][3][0]), "+v"(A[0][3][1]), "+v"(A[0][4][0]), "+v"(A[0][4][1]), "+v"(A[0][5][0]), "+v"(A[0][5][1]) :: "memory");
    MF(C[0][0][0], W[0], A[0][0][0]);
    SB;
    W[1][0] = PERM_E(R[0][1][2], R[0][0][2]);
    W[1][1] = PERM_E(R[0][3][3], R[0][2][3]);
    SB;
    MF(C[1][0][0], W[0], A[0][1][0]);
    SB;
    W[1][2] = PERM_E(R[0][6][0], R[0][5][0]);
    W[1][3] = PERM_E(R[0][8][1], R[0][7][1]);
    SB;
    MF(C[2][0][0], W[0], A[0][2][0]);
    SB;
    SL[0][0] = SHR1(A[0][0][1][0]);
    SL[0][1] = SHR1(A[0][0][1][1]);
    SB;
    MF(C[3][0][0], W[0], A[0][3][0]);
    SB;
    ALOAD(A[1][0][0], va[0], sa3, 0);
    SB;
    MF(C[0][1][0], W[0], A[0][0][1]);
    SB;
    SL[0][2] = SHR1(A[0][0][1][2]);
    SL[0][3] = SHR1(A[0][0][1][3]);
    SB;
    MF(C[1][1][0], W[0], A[0][1][1]);
    SB;
    SL[1][0] = SHR1(A[0][1][1][0]);
    SL[1][1] = SHR1(A[0][1][1][1]);
    SB;
    MF(C[2][1][0], W[0], A[0][2][1]);
    SB;
    SL[1][2] = SHR1(A[0][1][1][2]);
    SL[1][3] = SHR1(A[0][1][1][3]);
    SB;
    MF(C[3][1][0], W[0], A[0][3][1]);
    SB;
    ALOAD(A[1][0][1], va[0], sa3, 1024);
    SB;
    MF(C[0][0][0], W[1], A[0][1][0]);
    SB;
    W[0][0] = PERM_O(R[0][2][0], R[0][0][3]);
    W[0][1] = PERM_O(R[0][4][1], R[0][3][0]);
    SB;
    MF(C[1][0][0], W[1], A[0][2][0]);
    SB;
    W[0][2] = PERM_O(R[0][6][2], R[0][5][1]);
    W[0][3] = PERM_O(R[0][8][3], R[0][7][2]);
    SB;
    MF(C[2][0][0], W[1], A[0][3][0]);
    SB;
    SL[2][0] = SHR1(A[0][2][1][0]);
    SL[2][1] = SHR1(A[0][2][1][1]);
    SB;
    MF(C[3][0][0], W[1], A[0][4][0]);
    SB;
    ALOAD(A[1][1][0], va[1], sa3, 0);
    SB;
    MF(C[0][1][0], W[1], A[0][1][1]);
    SB;
    SL[2][2] = SHR1(A[0][2][1][2]);
    SL[2][3] = SHR1(A[0][2][1][3]);
    SB;
    MF(C[1][1][0], W[1], A[0][2][1]);
    SB;
    SL[3][0] = SHR1(A[0][3][1][0]);
    SL[3][1] = SHR1(A[0][3][1][1]);
    SB;
    MF(C[2][1][0], W[1], A[0][3][1]);
    SB;
    SL[3][2] = SHR1(A[0][3][1][2]);
    SL[3][3] = SHR1(A[0][3][1][3]);
    SB;
    MF(C[3][1][0], W[1], A[0][4][1]);
    SB;
    ALOAD(A[1][1][1], va[1], sa3, 1024);
    SB;
    MF(C[0][0][0], W[0], A[0][2][0]);
    SB;
    W[1][0] = PERM_E(R[0][1][0], R[0][0][0]);
    W[1][1] = PERM_E(R[0][3][1], R[0][2][1]);
    SB;
    MF(C[1][0][0], W[0], A[0][3][0]);
    SB;
    W[1][2] = PERM_E(R[0][5][2], R[0][4][2]);
    W[1][3] = PERM_E(R[0][7][3], R[0][6][3]);
    SB;
    MF(C[2][0][0], W[0], A[0][4][0]);
    SB;
    SL[4][0] = SHR1(A[0][4][1][0]);
    SL[4][1] = SHR1(A[0][4][1][1]);
    SB;
    MF(C[3][0][0], W[0], A[0][5][0]);
    SB;
    ALOAD(A[1][2][0], va[2], sa3, 0);
    SB;
    MF(C[0][1][0], W[0], A[0][2][1]);
    SB;
    SL[4][2] = SHR1(A[0][4][1][2]);
    SL[4][3] = SHR1(A[0][4][1][3]);
    SB;
    MF(C[1][1][0], W[0], A[0][3][1]);
    SB;
    SL[5][0] = SHR1(A[0][5][1][0]);
    SL[5][1] = SHR1(A[0][5][1][1]);
    SB;
    MF(C[2][1][0], W[0], A[0][4][1]);
    SB;
    SL[5][2] = SHR1(A[0][5][1][2]);
    SL[5][3] = SHR1(A[0][5][1][3]);
    SB;
    MF(C[3][1][0], W[0], A[0][5][1]);
    SB;
    ALOAD(A[1][2][1], va[2], sa3, 1024);
    SB;
    MF(C[0][1][0], W[1], A[0][0][0]);
    SB;
    W[0][0] = PERM_O(R[0][1][2], R[0][0][1]);
    W[0][1] = PERM_O(R[0][3][3], R[0][2][2]);
    SB;
    MF(C[1][1][0], W[1], A[0][1][0]);
    SB;
    W[0][2] = PERM_O(R[0][6][0], R[0][4][3]);
    W[0][3] = PERM_O(R[0][8][1], R[0][7][0]);
    SB;
    MF(C[2][1][0], W[1], A[0][2][0]);
    SB;
    SR[0][0] = SHL1(A[0][0][0][0]);
    SR[0][1] = SHL1(A[0][0][0][1]);
    SB;
    MF(C[3][1][0], W[1], A[0][3][0]);
    SB;
    ALOAD(A[1][3][0], va[3], sa3, 0);
    SB;
    MF(C[0][0][0], W[1], SL[0]);
    SB;
    SR[0][2] = SHL1(A[0][0][0][2]);
    SR[0][3] = SHL1(A[0][0][0][3]);
    SB;
    MF(C[1][0][0], W[1], SL[1]);
    SB;
    SR[1][0] = SHL1(A[0][1][0][0]);
    SR[1][1] = SHL1(A[0][1][0][1]);
    SB;
    MF(C[2][0][0], W[1], SL[2]);
    SB;
    SR[1][2] = SHL1(A[0][1][0][2]);
    SR[1][3] = SHL1(A[0][1][0][3]);
    SB;
    MF(C[3][0][0], W[1], SL[3]);
    SB;
    ALOAD(A[1][3][1], va[3], sa3, 1024);
    SB;
    MF(C[0][1][0], W[0], A[0][1][0]);
    SB;
    W[1][0] = PERM_E(R[0][1][3], R[0][0][3]);
    W[1][1] = PERM_E(R[0][4][0], R[0][3][0]);
    SB;
    MF(C[1][1][0], W[0], A[0][2][0]);
    SB;
    W[1][2] = PERM_E(R[0][6][1], R[0][5][1]);
    W[1][3] = PERM_E(R[0][8][2], R[0][7][2]);
    SB;
    MF(C[2][1][0], W[0], A[0][3][0]);
    SB;
    SR[2][0] = SHL1(A[0][2][0][0]);
    SR[2][1] = SHL1(A[0][2][0][1]);
    SB;
    MF(C[3][1][0], W[0], A[0][4][0]);
    SB;
    ALOAD(A[1][4][0], va[4], sa3, 0);
    SB;
    MF(C[0][0][0], W[0], SL[1]);
    SB;
    SR[2][2] = SHL1(A[0][2][0][2]);
    SR[2][3] = SHL1(A[0][2][0][3]);
    SB;
    MF(C[1][0][0], W[0], SL[2]);
    SB;
    SR[3][0] = SHL1(A[0][3][0][0]);
    SR[3][1] = SHL1(A[0][3][0][1]);
    SB;
    MF(C[2][0][0], W[0], SL[3]);
    SB;
    SR[3][2] = SHL1(A[0][3][0][2]);
    SR[3][3] = SHL1(A[0][3][0][3]);
    SB;
    MF(C[3][0][0], W[0], SL[4]);
    SB;
    ALOAD(A[1][4][1], va[4], sa3, 1024);
    SB;
    MF(C[0][1][0], W[1], A[0][2][0]);
    SB;
    W[0][0] = PERM_E(R[0][1][1], R[0][0][1]);
    W[0][1] = PERM_E(R[0][3][2], R[0][2][2]);
    SB;
    MF(C[1][1][0], W[1], A[0][3][0]);
    SB;
    W[0][2] = PERM_E(R[0][5][3], R[0][4][3]);
    W[0][3] = PERM_E(R[0][8][0], R[0][7][0]);
    SB;
    MF(C[2][1][0], W[1], A[0][4][0]);
    SB;
    SR[4][0] = SHL1(A[0][4][0][0]);
    SR[4][1] = SHL1(A[0][4][0][1]);
    SB;
    MF(C[3][1][0], W[1], A[0][5][0]);
    SB;
    ALOAD(A[1][5][0], va[5], sa3, 0);
    SB;
    MF(C[0][0][0], W[1], SL[2]);
    SB;
    SR[4][2] = SHL1(A[0][4][0][2]);
    SR[4][3] = SHL1(A[0][4][0][3]);
    SB;
    MF(C[1][0][0], W[1], SL[3]);
    SB;
    SR[5][0] = SHL1(A[0][5][0][0]);
    SR[5][1] = SHL1(A[0][5][0][1]);
    SB;
    MF(C[2][0][0], W[1], SL[4]);
    SB;
    SR[5][2] = SHL1(A[0][5][0][2]);
    SR[5][3] = SHL1(A[0][5][0][3]);
    SB;
    MF(C[3][0][0], W[1], SL[5]);
    SB;
    ALOAD(A[1][5][1], va[5], sa3, 1024);
    SB;
    MF(C[0][0][0], W[0], A[0][0][1]);
    SB;
    W[1][0] = PERM_O(R[0][1][3], R[0][0][2]);
    W[1][1] = PERM_O(R[0][4][0], R[0][2][3]);
    SB;
    MF(C[1][0][0], W[0], A[0][1][1]);
    SB;
    W[1][2] = PERM_O(R[0][6][1], R[0][5][0]);
    W[1][3] = PERM_O(R[0][8][2], R[0][7][1]);
    SB;
    MF(C[2][0][0], W[0], A[0][2][1]);
    SB;
    asm volatile("s_waitcnt vmcnt(12)" ::: "memory");
    R[1][0] = LDSR(ra[0], 18432);
    SB;
    MF(C[3][0][0], W[0], A[0][3][1]);
    SB;
    R[1][1] = LDSR(ra[1], 18432);
    POISON(0);
    SB;
    MF(C[0][1][0], W[0], SR[0]);
    SB;
    R[1][2] = LDSR(ra[2], 18432);
    SB;
    MF(C[1][1][0], W[0], SR[1]);
    SB;
    R[1][3] = LDSR(ra[3], 18432);
    SB;
    MF(C[2][1][0], W[0], SR[2]);
    SB;
    R[1][4] = LDSR(ra[4], 18432);
    SB;
    MF(C[3][1][0], W[0], SR[3]);
    SB;
    R[1][5] = LDSR(ra[5], 18432);
    POISON(2048);
    SB;
    MF(C[0][0][0], W[1], A[0][1][1]);
    SB;
    R[1][6] = LDSR(ra[6], 18432);
    W[0][0] = PERM_E(R[0][2][0], R[0][1][0]);
    SB;
    MF(C[1][0][0], W[1], A[0][2][1]);
    SB;
    R[1][7] = LDSR(ra[7], 18432);
    W[0][1] = PERM_E(R[0][4][1], R[0][3][1]);
    SB;
    MF(C[2][0][0], W[1], A[0][3][1]);
    SB;
    R[1][8] = LDSR(ra[0], 18560);
    W[0][2] = PERM_E(R[0][6][2], R[0][5][2]);
    SB;
    MF(C[3][0][0], W[1], A[0][4][1]);
    SB;
    ELOAD();
    SB;
    MF(C[0][1][0], W[1], SR[1]);
    SB;
    W[0][3] = PERM_E(R[0][8][3], R[0][7][3]);
    SB;
    MF(C[1][1][0], W[1], SR[2]);
    SB;
    MF(C[2][1][0], W[1], SR[3]);
    SB;
    MF(C[3][1][0], W[1], SR[4]);
    SB;
    MF(C[0][0][0], W[0], A[0][2][1]);
    SB;
    W[1][0] = PERM_E(R[1][1][0], R[1][0][0]);
    W[1][1] = PERM_E(R[1][3][1], R[1][2][1]);
    SB;
    MF(C[1][0][0], W[0], A[0][3][1]);
    SB;
    W[1][2] = PERM_E(R[1][5][2], R[1][4][2]);
    W[1][3] = PERM_E(R[1][7][3], R[1][6][3]);
    SB;
    MF(C[2][0][0], W[0], A[0][4][1]);
    SB;
    MF(C[3][0][0], W[0], A[0][5][1]);
    SB;
    MF(C[0][1][0], W[0], SR[2]);
    SB;
    MF(C[1][1][0], W[0], SR[3]);
    SB;
    MF(C[2][1][0], W[0], SR[4]);
    SB;
    MF(C[3][1][0], W[0], SR[5]);
    SB;
    MF(C[0][1][1], W[1], A[0][0][0]);
    SB;
    W[0][0] = PERM_O(R[1][1][1], R[1][0][0]);
    W[0][1] = PERM_O(R[1][3][2], R[1][2][1]);
    SB;
    MF(C[1][1][1], W[1], A[0][1][0]);
    SB;
    W[0][2] = PERM_O(R[1][5][3], R[1][4][2]);
    W[0][3] = PERM_O(R[1][8][0], R[1][6][3]);
    SB;
    MF(C[2][1][1], W[1], A[0][2][0]);
    SB;
    MF(C[3][1][1], W[1], A[0][3][0]);
    SB;
    MF(C[0][0][1], W[1], SL[0]);
    SB;
    R[0][0] = LDSR(ra[1], 128);
    SB;
    MF(C[1][0][1], W[1], SL[1]);
    SB;
    R[0][1] = LDSR(ra[2], 128);
    SB;
    MF(C[2][0][1], W[1], SL[2]);
    SB;
    R[0][2] = LDSR(ra[3], 128);
    SB;
    MF(C[3][0][1], W[1], SL[3]);
    SB;
    R[0][3] = LDSR(ra[4], 128);
    SB;
    MF(C[0][0][1], W[0], A[0][0][0]);
    SB;
    R[0][4] = LDSR(ra[5], 128);
    W[1][0] = PERM_E(R[1][1][1], R[1][0][1]);
    SB;
    MF(C[1][0][1], W[0], A[0][1][0]);
    SB;
    R[0][5] = LDSR(ra[6], 128);
    W[1][1] = PERM_E(R[1][3][2], R[1][2][2]);
    SB;
    MF(C[2][0][1], W[0], A[0][2][0]);
    SB;
    R[0][6] = LDSR(ra[7], 128);
    W[1][2] = PERM_E(R[1][5][3], R[1][4][3]);
    SB;
    MF(C[3][0][1], W[0], A[0][3][0]);
    SB;
    R[0][7] = LDSR(ra[0], 256);
    W[1][3] = PERM_E(R[1][8][0], R[1][7][0]);
    SB;
    MF(C[0][1][1], W[0], A[0][0][1]);
    SB;
    R[0][8] = LDSR(ra[1], 256);
    SB;
    MF(C[1][1][1], W[0], A[0][1][1]);
    SB;
    MF(C[2][1][1], W[0], A[0][2][1]);
    SB;
    MF(C[3][1][1], W[0], A[0][3][1]);
    SB;
    MF(C[0][0][1], W[1], A[0][0][1]);
    SB;
    W[0][0] = PERM_O(R[1][1][2], R[1][0][1]);
    W[0][1] = PERM_O(R[1][3][3], R[1][2][2]);
    SB;
    MF(C[1][0][1], W[1], A[0][1][1]);
    SB;
    W[0][2] = PERM_O(R[1][6][0], R[1][4][3]);
    W[0][3] = PERM_O(R[1][8][1], R[1][7][0]);
    SB;
    MF(C[2][0][1], W[1], A[0][2][1]);
    SB;
    MF(C[3][0][1], W[1], A[0][3][1]);
    SB;
    MF(C[0][1][1], W[1], SR[0]);
    SB;
    MF(C[1][1][1], W[1], SR[1]);
    SB;
    PIN9(R[0]);
    SETM0N(m0h0, 0);
    SB;
    MF(C[2][1][1], W[1], SR[2]);
    SB;
    DMA(srdWn, vw[0], sw_0_0_0, 0);
    SB;
    MF(C[3][1][1], W[1], SR[3]);
    SB;
    MF(C[0][1][1], W[0], A[0][1][0]);
    SB;
    W[1][0] = PERM_E(R[1][1][2], R[1][0][2]);
    W[1][1] = PERM_E(R[1][3][3], R[1][2][3]);
    SB;
    MF(C[1][1][1], W[0], A[0][2][0]);
    SB;
    W[1][2] = PERM_E(R[1][6][0], R[1][5][0]);
    W[1][3] = PERM_E(R[1][8][1], R[1][7][1]);
    SB;
    MF(C[2][1][1], W[0], A[0][3][0]);
    SB;
    DMA(srdWn, vw[1], sw_0_0_0, 1024);
    SB;
    MF(C[3][1][1], W[0], A[0][4][0]);
    SB;
    MF(C[0][0][1], W[0], SL[1]);
    SB;
    MF(C[1][0][1], W[0], SL[2]);
    SB;
    MF(C[2][0][1], W[0], SL[3]);
    SB;
    DMA(srdWn, vw[2], sw_0_0_0, 2048);
    SB;
    MF(C[3][0][1], W[0], SL[4]);
    SB;
    MF(C[0][0][1], W[1], A[0][1][0]);
    SB;
    W[0][0] = PERM_O(R[1][1][3], R[1][0][2]);
    W[0][1] = PERM_O(R[1][4][0], R[1][2][3]);
    SB;
    MF(C[1][0][1], W[1], A[0][2][0]);
    SB;
    W[0][2] = PERM_O(R[1][6][1], R[1][5][0]);
    W[0][3] = PERM_O(R[1][8][2], R[1][7][1]);
    SB;
    MF(C[2][0][1], W[1], A[0][3][0]);
    SB;
    DMA(srdWn, vw[3], sw_0_0_0, 3072);
    SB;
    MF(C[3][0][1], W[1], A[0][4][0]);
    SB;
    MF(C[0][1][1], W[1], A[0][1][1]);
    SB;
    MF(C[1][1][1], W[1], A[0][2][1]);
    SB;
    SETM0N(m0h0, 4096);
    SB;
    MF(C[2][1][1], W[1], A[0][3][1]);
    SB;
    DMA(srdWn, vw[4], sw_0_0_0, 0);
    SB;
    MF(C[3][1][1], W[1], A[0][4][1]);
    SB;
    MF(C[0][0][1], W[0], A[0][1][1]);
    SB;
    W[1][0] = PERM_E(R[1][1][3], R[1][0][3]);
    W[1][1] = PERM_E(R[1][4][0], R[1][3][0]);
    SB;
    MF(C[1][0][1], W[0], A[0][2][1]);
    SB;
    W[1][2] = PERM_E(R[1][6][1], R[1][5][1]);
    W[1][3] = PERM_E(R[1][8][2], R[1][7][2]);
    SB;
    MF(C[2][0][1], W[0], A[0][3][1]);
    SB;
    DMA(srdWn, vw[5], sw_0_0_0, 1024);
    SB;
    MF(C[3][0][1], W[0], A[0][4][1]);
    SB;
    MF(C[0][1][1], W[0], SR[1]);
    SB;
    asm volatile("s_waitcnt vmcnt(6)" : "+a"(EG) :: "memory");
    SB;
    MF(C[1][1][1], W[0], SR[2]);
    SB;
    EMB0();
    SB;
    MF(C[2][1][1], W[0], SR[3]);
    SB;
    DMA(srdWn, vw[6], sw_0_0_0, 2048);
    SB;
    MF(C[3][1][1], W[0], SR[4]);
    SB;
    EMB1();
    SB;
    MF(C[0][1][1], W[1], A[0][2][0]);
    SB;
    W[0][0] = PERM_O(R[1][2][0], R[1][0][3]);
    W[0][1] = PERM_O(R[1][4][1], R[1][3][0]);
    SB;
    MF(C[1][1][1], W[1], A[0][3][0]);
    SB;
    W[0][2] = PERM_O(R[1][6][2], R[1][5][1]);
    W[0][3] = PERM_O(R[1][8][3], R[1][7][2]);
    SB;
    MF(C[2][1][1], W[1], A[0][4][0]);
    SB;
    DMA(srdWn, vw[7], sw_0_0_0, 3072);
    SB;
    MF(C[3][1][1], W[1], A[0][5][0]);
    SB;
    EMB2();
    SB;
    MF(C[0][0][1], W[1], SL[2]);
    SB;
    MF(C[1][0][1], W[1], SL[3]);
    SB;
    SETM0N(m0h0, 8192);
    SB;
    MF(C[2][0][1], W[1], SL[4]);
    SB;
    DMA(srdWn, vw[8], sw_0_0_0, 0);
    SB;
    MF(C[3][0][1], W[1], SL[5]);
    SB;
    EMB3();
    SB;
    MF(C[0][0][1], W[0], A[0][2][0]);
    SB;
    W[1][0] = PERM_E(R[1][2][0], R[1][1][0]);
    W[1][1] = PERM_E(R[1][4][1], R[1][3][1]);
    SB;
    MF(C[1][0][1], W[0], A[0][3][0]);
    SB;
    W[1][2] = PERM_E(R[1][6][2], R[1][5][2]);
    W[1][3] = PERM_E(R[1][8][3], R[1][7][3]);
    SB;
    MF(C[2][0][1], W[0], A[0][4][0]);
    SB;
    EMB4();
    SB;
    MF(C[3][0][1], W[0], A[0][5][0]);
    SB;
    MF(C[0][1][1], W[0], A[0][2][1]);
    SB;
    EMB5();
    SB;
    MF(C[1][1][1], W[0], A[0][3][1]);
    SB;
    MF(C[2][1][1], W[0], A[0][4][1]);
    SB;
    EMB6();
    SB;
    MF(C[3][1][1], W[0], A[0][5][1]);
    SB;
    MF(C[0][0][1], W[1], A[0][2][1]);
    SB;
    W[0][0] = PERM_O(R[0][5][3], R[0][4][2]);
    W[0][1] = PERM_O(R[0][8][0], R[0][6][3]);
    SB;
    MF(C[1][0][1], W[1], A[0][3][1]);
    SB;
    W[0][2] = PERM_O(R[0][1][1], R[0][0][0]);
    W[0][3] = PERM_O(R[0][3][2], R[0][2][1]);
    SB;
    MF(C[2][0][1], W[1], A[0][4][1]);
    SB;
    EMB7();
    SB;
    MF(C[3][0][1], W[1], A[0][5][1]);
    SB;
    MF(C[0][1][1], W[1], SR[2]);
    SB;
    EMB8();
    SB;
    MF(C[1][1][1], W[1], SR[3]);
    SB;
    MF(C[2][1][1], W[1], SR[4]);
    SB;
    MF(C[3][1][1], W[1], SR[5]);
    SB;
    asm volatile("s_waitcnt vmcnt(12)" : "+v"(A[1][0][0]), "+v"(A[1][0][1]), "+v"(A[1][1][0]), "+v"(A[1][1][1]), "+v"(A[1][2][0]), "+v"(A[1][2][1]), "+v"(A[1][3][0]), "+v"(A[1][3][1]), "+v"(A[1][4][0]), "+v"(A[1][4][1]), "+v"(A[1][5][0]), "+v"(A[1][5][1]) :: "memory");
    MF(C[0][0][0], W[0], A[1][0][0]);
    SB;
    W[1][0] = PERM_E(R[0][6][0], R[0][5][0]);
    W[1][1] = PERM_E(R[0][8][1], R[0][7][1]);
    SB;
    MF(C[1][0][0], W[0], A[1][1][0]);
    SB;
    W[1][2] = PERM_E(R[0][1][2], R[0][0][2]);
    W[1][3] = PERM_E(R[0][3][3], R[0][2][3]);
    SB;
    MF(C[2][0][0], W[0], A[1][2][0]);
    SB;
    SL[0][0] = SHR1(A[1][0][1][0]);
    SL[0][1] = SHR1(A[1][0][1][1]);
    SB;
    MF(C[3][0][0], W[0], A[1][3][0]);
    SB;
    SL[0][2] = SHR1(A[1][0][1][2]);
    SL[0][3] = SHR1(A[1][0][1][3]);
    SB;
    MF(C[0][1][0], W[0], A[1][0][1]);
    SB;
    R[1][0] = LDSR(ra[1], 18560);
    SL[1][0] = SHR1(A[1][1][1][0]);
    SB;
    MF(C[1][1][0], W[0], A[1][1][1]);
    SB;
    R[1][1] = LDSR(ra[2], 18560);
    SL[1][1] = SHR1(A[1][1][1][1]);
    SB;
    MF(C[2][1][0], W[0], A[1][2][1]);
    SB;
    R[1][2] = LDSR(ra[3], 18560);
    SL[1][2] = SHR1(A[1][1][1][2]);
    SB;
    MF(C[3][1][0], W[0], A[1][3][1]);
    SB;
    R[1][3] = LDSR(ra[4], 18560);
    SL[1][3] = SHR1(A[1][1][1][3]);
    SB;
    MF(C[0][0][0], W[1], A[1][1][0]);
    SB;
    R[1][4] = LDSR(ra[5], 18560);
    W[0][0] = PERM_O(R[0][6][2], R[0][5][1]);
    SB;
    MF(C[1][0][0], W[1], A[1][2][0]);
    SB;
    R[1][5] = LDSR(ra[6], 18560);
    W[0][1] = PERM_O(R[0][8][3], R[0][7][2]);
    SB;
    MF(C[2][0][0], W[1], A[1][3][0]);
    SB;
    R[1][6] = LDSR(ra[7], 18560);
    W[0][2] = PERM_O(R[0][2][0], R[0][0][3]);
    SB;
    MF(C[3][0][0], W[1], A[1][4][0]);
    SB;
    R[1][7] = LDSR(ra[0], 18688);
    W[0][3] = PERM_O(R[0][4][1], R[0][3][0]);
    SB;
    MF(C[0][1][0], W[1], A[1][1][1]);
    SB;
    R[1][8] = LDSR(ra[1], 18688);
    SL[2][0] = SHR1(A[1][2][1][0]);
    SB;
    MF(C[1][1][0], W[1], A[1][2][1]);
    SB;
    SL[2][1] = SHR1(A[1][2][1][1]);
    SL[2][2] = SHR1(A[1][2][1][2]);
    SB;
    MF(C[2][1][0], W[1], A[1][3][1]);
    SB;
    SL[2][3] = SHR1(A[1][2][1][3]);
    SL[3][0] = SHR1(A[1][3][1][0]);
    SB;
    MF(C[3][1][0], W[1], A[1][4][1]);
    SB;
    SL[3][1] = SHR1(A[1][3][1][1]);
    SL[3][2] = SHR1(A[1][3][1][2]);
    SB;
    MF(C[0][0][0], W[0], A[1][2][0]);
    SB;
    W[1][0] = PERM_E(R[0][5][2], R[0][4][2]);
    W[1][1] = PERM_E(R[0][7][3], R[0][6][3]);
    SB;
    MF(C[1][0][0], W[0], A[1][3][0]);
    SB;
    W[1][2] = PERM_E(R[0][1][0], R[0][0][0]);
    W[1][3] = PERM_E(R[0][3][1], R[0][2][1]);
    SB;
    MF(C[2][0][0], W[0], A[1][4][0]);
    SB;
    SL[3][3] = SHR1(A[1][3][1][3]);
    SL[4][0] = SHR1(A[1][4][1][0]);
    SB;
    MF(C[3][0][0], W[0], A[1][5][0]);
    SB;
    SETM0N(m0h1, 0);
    SL[4][1] = SHR1(A[1][4][1][1]);
    SB;
    MF(C[0][1][0], W[0], A[1][2][1]);
    SB;
    DMA(srdWn, vw[0], sw_0_0_1, 0);
    SB;
    MF(C[1][1][0], W[0], A[1][3][1]);
    SB;
    PIN9(R[1]);
    SL[4][2] = SHR1(A[1][4][1][2]);
    SL[4][3] = SHR1(A[1][4][1][3]);
    WF[7][2] = PERM_O(R[1][2][0], R[1][0][3]);
    SB;
    MF(C[2][1][0], W[0], A[1][4][1]);
    SB;
    SL[5][0] = SHR1(A[1][5][1][0]);
    SL[5][1] = SHR1(A[1][5][1][1]);
    WF[7][3] = PERM_O(R[1][4][1], R[1][3][0]);
    SB;
    MF(C[3][1][0], W[0], A[1][5][1]);
    SB;
    CHKR(ck0, C[0][0][0]);
    SL[5][2] = SHR1(A[1][5][1][2]);
    WF[8][0] = PERM_E(R[1][6][2], R[1][5][2]);
    SB;
    MF(C[0][1][0], W[1], A[1][0][0]);
    SB;
    DMA(srdWn, vw[1], sw_0_0_1, 1024);
    WF[8][1] = PERM_E(R[1][8][3], R[1][7][3]);
    SB;
    MF(C[1][1][0], W[1], A[1][1][0]);
    SB;
    W[0][0] = PERM_O(R[0][6][0], R[0][4][3]);
    W[0][1] = PERM_O(R[0][8][1], R[0][7][0]);
    WF[8][2] = PERM_E(R[1][2][0], R[1][1][0]);
    SB;
    MF(C[2][1][0], W[1], A[1][2][0]);
    SB;
    W[0][2] = PERM_O(R[0][1][2], R[0][0][1]);
    W[0][3] = PERM_O(R[0][3][3], R[0][2][2]);
    WF[8][3] = PERM_E(R[1][4][1], R[1][3][1]);
    SB;
    MF(C[3][1][0], W[1], A[1][3][0]);
    SB;
    CHKR(ck1, C[3][0][0]);
    SL[5][3] = SHR1(A[1][5][1][3]);
    SB;
    MF(C[0][0][0], W[1], SL[0]);
    SB;
    DMA(srdWn, vw[2], sw_0_0_1, 2048);
    SB;
    MF(C[1][0][0], W[1], SL[1]);
    SB;
    CHKR(ck2, C[0][1][0]);
    SR[0][0] = SHL1(A[1][0][0][0]);
    SB;
    MF(C[2][0][0], W[1], SL[2]);
    SB;
    SR[0][1] = SHL1(A[1][0][0][1]);
    SR[0][2] = SHL1(A[1][0][0][2]);
    SB;
    MF(C[3][0][0], W[1], SL[3]);
    SB;
    CHKR(ck3, C[3][1][0]);
    SR[0][3] = SHL1(A[1][0][0][3]);
    SB;
    MF(C[0][1][0], W[0], A[1][1][0]);
    SB;
    DMA(srdWn, vw[3], sw_0_0_1, 3072);
    SB;
    MF(C[1][1][0], W[0], A[1][2][0]);
    SB;
    W[1][0] = PERM_E(R[0][6][1], R[0][5][1]);
    W[1][1] = PERM_E(R[0][8][2], R[0][7][2]);
    SB;
    MF(C[2][1][0], W[0], A[1][3][0]);
    SB;
    W[1][2] = PERM_E(R[0][1][3], R[0][0][3]);
    W[1][3] = PERM_E(R[0][4][0], R[0][3][0]);
    CHK(1);
    SB;
    MF(C[3][1][0], W[0], A[1][4][0]);
    SB;
    SETM0N(m0h1, 4096);
    SR[1][0] = SHL1(A[1][1][0][0]);
    WF[7][0] = PERM_O(R[1][6][2], R[1][5][1]);
    SB;
    MF(C[0][0][0], W[0], SL[1]);
    SB;
    DMA(srdWn, vw[4], sw_0_0_1, 0);
    SB;
    MF(C[1][0][0], W[0], SL[2]);
    SB;
    SR[1][1] = SHL1(A[1][1][0][1]);
    SR[1][2] = SHL1(A[1][1][0][2]);
    SB;
    MF(C[2][0][0], W[0], SL[3]);
    SB;
    SR[1][3] = SHL1(A[1][1][0][3]);
    SR[2][0] = SHL1(A[1][2][0][0]);
    SB;
    MF(C[3][0][0], W[0], SL[4]);
    SB;
    SR[2][1] = SHL1(A[1][2][0][1]);
    SR[2][2] = SHL1(A[1][2][0][2]);
    SB;
    MF(C[0][1][0], W[1], A[1][2][0]);
    SB;
    DMA(srdWn, vw[5], sw_0_0_1, 1024);
    SB;
    MF(C[1][1][0], W[1], A[1][3][0]);
    SB;
    W[0][0] = PERM_E(R[0][5][3], R[0][4][3]);
    W[0][1] = PERM_E(R[0][8][0], R[0][7][0]);
    SB;
    MF(C[2][1][0], W[1], A[1][4][0]);
    SB;
    W[0][2] = PERM_E(R[0][1][1], R[0][0][1]);
    W[0][3] = PERM_E(R[0][3][2], R[0][2][2]);
    SB;
    MF(C[3][1][0], W[1], A[1][5][0]);
    SB;
    SR[2][3] = SHL1(A[1][2][0][3]);
    SR[3][0] = SHL1(A[1][3][0][0]);
    SB;
    MF(C[0][0][0], W[1], SL[2]);
    SB;
    DMA(srdWn, vw[6], sw_0_0_1, 2048);
    SB;
    MF(C[1][0][0], W[1], SL[3]);
    SB;
    SR[3][1] = SHL1(A[1][3][0][1]);
    SR[3][2] = SHL1(A[1][3][0][2]);
    SB;
    MF(C[2][0][0], W[1], SL[4]);
    SB;
    SR[3][3] = SHL1(A[1][3][0][3]);
    SR[4][0] = SHL1(A[1][4][0][0]);
    SB;
    MF(C[3][0][0], W[1], SL[5]);
    SB;
    SR[4][1] = SHL1(A[1][4][0][1]);
    SR[4][2] = SHL1(A[1][4][0][2]);
    SB;
    MF(C[0][0][0], W[0], A[1][0][1]);
    SB;
    DMA(srdWn, vw[7], sw_0_0_1, 3072);
    SB;
    MF(C[1][0][0], W[0], A[1][1][1]);
    SB;
    W[1][0] = PERM_O(R[0][6][1], R[0][5][0]);
    W[1][1] = PERM_O(R[0][8][2], R[0][7][1]);
    SB;
    MF(C[2][0][0], W[0], A[1][2][1]);
    SB;
    W[1][2] = PERM_O(R[0][1][3], R[0][0][2]);
    W[1][3] = PERM_O(R[0][4][0], R[0][2][3]);
    SB;
    MF(C[3][0][0], W[0], A[1][3][1]);
    SB;
    SETM0N(m0h1, 8192);
    SR[4][3] = SHL1(A[1][4][0][3]);
    WF[7][1] = PERM_O(R[1][8][3], R[1][7][2]);
    SB;
    MF(C[0][1][0], W[0], SR[0]);
    SB;
    DMA(srdWn, vw[8], sw_0_0_1, 0);
    SB;
    MF(C[1][1][0], W[0], SR[1]);
    SB;
    SR[5][0] = SHL1(A[1][5][0][0]);
    SR[5][1] = SHL1(A[1][5][0][1]);
    SB;
    MF(C[2][1][0], W[0], SR[2]);
    SB;
    SR[5][2] = SHL1(A[1][5][0][2]);
    SR[5][3] = SHL1(A[1][5][0][3]);
    SB;
    MF(C[3][1][0], W[0], SR[3]);
    SB;
    WF[0][0] = PERM_E(R[1][5][2], R[1][4][2]);
    WF[0][1] = PERM_E(R[1][7][3], R[1][6][3]);
    SB;
    MF(C[0][0][0], W[1], A[1][1][1]);
    SB;
    W[0][0] = PERM_E(R[0][6][2], R[0][5][2]);
    W[0][1] = PERM_E(R[0][8][3], R[0][7][3]);
    SB;
    MF(C[1][0][0], W[1], A[1][2][1]);
    SB;
    W[0][2] = PERM_E(R[0][2][0], R[0][1][0]);
    W[0][3] = PERM_E(R[0][4][1], R[0][3][1]);
    SB;
    MF(C[2][0][0], W[1], A[1][3][1]);
    SB;
    WF[0][2] = PERM_E(R[1][1][0], R[1][0][0]);
    WF[0][3] = PERM_E(R[1][3][1], R[1][2][1]);
    SB;
    MF(C[3][0][0], W[1], A[1][4][1]);
    SB;
    WF[1][0] = PERM_O(R[1][5][3], R[1][4][2]);
    WF[1][1] = PERM_O(R[1][8][0], R[1][6][3]);
    SB;
    MF(C[0][1][0], W[1], SR[1]);
    SB;
    WF[1][2] = PERM_O(R[1][1][1], R[1][0][0]);
    WF[1][3] = PERM_O(R[1][3][2], R[1][2][1]);
    SB;
    MF(C[1][1][0], W[1], SR[2]);
    SB;
    WF[2][0] = PERM_E(R[1][5][3], R[1][4][3]);
    WF[2][1] = PERM_E(R[1][8][0], R[1][7][0]);
    SB;
    MF(C[2][1][0], W[1], SR[3]);
    SB;
    WF[2][2] = PERM_E(R[1][1][1], R[1][0][1]);
    WF[2][3] = PERM_E(R[1][3][2], R[1][2][2]);
    SB;
    MF(C[3][1][0], W[1], SR[4]);
    SB;
    WF[3][0] = PERM_O(R[1][6][0], R[1][4][3]);
    WF[3][1] = PERM_O(R[1][8][1], R[1][7][0]);
    SB;
    MF(C[0][0][0], W[0], A[1][2][1]);
    SB;
    WF[3][2] = PERM_O(R[1][1][2], R[1][0][1]);
    WF[3][3] = PERM_O(R[1][3][3], R[1][2][2]);
    SB;
    MF(C[1][0][0], W[0], A[1][3][1]);
    SB;
    WF[4][0] = PERM_E(R[1][6][0], R[1][5][0]);
    WF[4][1] = PERM_E(R[1][8][1], R[1][7][1]);
    SB;
    MF(C[2][0][0], W[0], A[1][4][1]);
    SB;
    WF[4][2] = PERM_E(R[1][1][2], R[1][0][2]);
    WF[4][3] = PERM_E(R[1][3][3], R[1][2][3]);
    SB;
    MF(C[3][0][0], W[0], A[1][5][1]);
    SB;
    WF[5][0] = PERM_O(R[1][6][1], R[1][5][0]);
    WF[5][1] = PERM_O(R[1][8][2], R[1][7][1]);
    SB;
    MF(C[0][1][0], W[0], SR[2]);
    SB;
    WF[5][2] = PERM_O(R[1][1][3], R[1][0][2]);
    WF[5][3] = PERM_O(R[1][4][0], R[1][2][3]);
    SB;
    MF(C[1][1][0], W[0], SR[3]);
    SB;
    WF[6][0] = PERM_E(R[1][6][1], R[1][5][1]);
    WF[6][1] = PERM_E(R[1][8][2], R[1][7][2]);
    SB;
    MF(C[2][1][0], W[0], SR[4]);
    SB;
    WF[6][2] = PERM_E(R[1][1][3], R[1][0][3]);
    WF[6][3] = PERM_E(R[1][4][0], R[1][3][0]);
    SB;
    MF(C[3][1][0], W[0], SR[5]);
    SB;
    asm volatile("" : "=v"(Hn[0][0]), "=v"(Hn[0][1]), "=v"(Hn[1][0]), "=v"(Hn[1][1]));
    if (wpa == 0) {
    MF(C[2][0][1], WF[0], SL[2]);
    SB;
    ACCMV(hv[0][0][0], C[0][0][1][0]);
    SB;
    MF(C[2][1][1], WF[0], A[1][2][0]);
    SB;
    REDD(0, 0, 0, 0);
    ADD1(hv[0][0][0], ev[0]);
    SB;
    MF(C[3][0][1], WF[0], SL[3]);
    SB;
    REDD(0, 0, 0, 1);
    ACCMV(hv[0][0][1], C[0][0][1][1]);
    SB;
    MF(C[3][1][1], WF[0], A[1][3][0]);
    SB;
    REDD(0, 0, 0, 2);
    ADD1(hv[0][0][1], ev[1]);
    SB;
    MF(C[2][0][1], WF[1], A[1][2][0]);
    SB;
    REDD(0, 0, 0, 3);
    ACCMV(hv[0][0][2], C[0][0][1][2]);
    SB;
    MF(C[2][1][1], WF[1], A[1][2][1]);
    SB;
    REDD(0, 1, 0, 0);
    ADD1(hv[0][0][2], ev[2]);
    SB;
    MF(C[3][0][1], WF[1], A[1][3][0]);
    SB;
    REDD(0, 1, 0, 1);
    ACCMV(hv[0][0][3], C[0][0][1][3]);
    SB;
    MF(C[3][1][1], WF[1], A[1][3][1]);
    SB;
    REDD(0, 1, 0, 2);
    ADD1(hv[0][0][3], ev[3]);
    SB;
    MF(C[2][0][1], WF[2], A[1][2][1]);
    SB;
    REDD(0, 1, 0, 3);
    ACCMV(hv[1][0][0], C[0][1][1][0]);
    SB;
    MF(C[2][1][1], WF[2], SR[2]);
    SB;
    REDD(1, 0, 0, 0);
    ADD1(hv[1][0][0], ev[0]);
    SB;
    MF(C[3][0][1], WF[2], A[1][3][1]);
    SB;
    REDD(1, 0, 0, 1);
    ACCMV(hv[1][0][1], C[0][1][1][1]);
    SB;
    MF(C[3][1][1], WF[2], SR[3]);
    SB;
    REDD(1, 0, 0, 2);
    ADD1(hv[1][0][1], ev[1]);
    SB;
    MF(C[2][0][1], WF[3], SL[3]);
    SB;
    REDD(1, 0, 0, 3);
    ACCMV(hv[1][0][2], C[0][1][1][2]);
    SB;
    MF(C[2][1][1], WF[3], A[1][3][0]);
    SB;
    REDD(1, 1, 0, 0);
    ADD1(hv[1][0][2], ev[2]);
    SB;
    MF(C[3][0][1], WF[3], SL[4]);
    SB;
    REDD(1, 1, 0, 1);
    ACCMV(hv[1][0][3], C[0][1][1][3]);
    SB;
    MF(C[3][1][1], WF[3], A[1][4][0]);
    SB;
    REDD(1, 1, 0, 2);
    ADD1(hv[1][0][3], ev[3]);
    SB;
    MF(C[2][0][1], WF[4], A[1][3][0]);
    SB;
    REDD(1, 1, 0, 3);
    ACCMV(hv[0][1][0], C[1][0][1][0]);
    SB;
    MF(C[2][1][1], WF[4], A[1][3][1]);
    SB;
    REDD(2, 0, 0, 0);
    ADD1(hv[0][1][0], ev[0]);
    SB;
    MF(C[3][0][1], WF[4], A[1][4][0]);
    SB;
    REDD(2, 0, 0, 1);
    ACCMV(hv[0][1][1], C[1][0][1][1]);
    SB;
    MF(C[3][1][1], WF[4], A[1][4][1]);
    SB;
    REDD(2, 0, 0, 2);
    ADD1(hv[0][1][1], ev[1]);
    SB;
    MF(C[2][0][1], WF[5], A[1][3][1]);
    SB;
    REDD(2, 0, 0, 3);
    ACCMV(hv[0][1][2], C[1][0][1][2]);
    SB;
    MF(C[2][1][1], WF[5], SR[3]);
    SB;
    REDD(2, 1, 0, 0);
    ADD1(hv[0][1][2], ev[2]);
    SB;
    MF(C[3][0][1], WF[5], A[1][4][1]);
    SB;
    REDD(2, 1, 0, 1);
    ACCMV(hv[0][1][3], C[1][0][1][3]);
    SB;
    MF(C[3][1][1], WF[5], SR[4]);
    SB;
    REDD(2, 1, 0, 2);
    ADD1(hv[0][1][3], ev[3]);
    SB;
    MF(C[2][0][1], WF[6], SL[4]);
    SB;
    REDD(2, 1, 0, 3);
    ACCMV(hv[1][1][0], C[1][1][1][0]);
    SB;
    MF(C[2][1][1], WF[6], A[1][4][0]);
    SB;
    REDD(3, 0, 0, 0);
    ADD1(hv[1][1][0], ev[0]);
    SB;
    MF(C[3][0][1], WF[6], SL[5]);
    SB;
    REDD(3, 0, 0, 1);
    ACCMV(hv[1][1][1], C[1][1][1][1]);
    SB;
    MF(C[3][1][1], WF[6], A[1][5][0]);
    SB;
    REDD(3, 0, 0, 2);
    ADD1(hv[1][1][1], ev[1]);
    SB;
    MF(C[2][0][1], WF[7], A[1][4][0]);
    SB;
    REDD(3, 0, 0, 3);
    ACCMV(hv[1][1][2], C[1][1][1][2]);
    SB;
    MF(C[2][1][1], WF[7], A[1][4][1]);
    SB;
    REDD(3, 1, 0, 0);
    ADD1(hv[1][1][2], ev[2]);
    SB;
    MF(C[3][0][1], WF[7], A[1][5][0]);
    SB;
    REDD(3, 1, 0, 1);
    ACCMV(hv[1][1][3], C[1][1][1][3]);
    SB;
    MF(C[3][1][1], WF[7], A[1][5][1]);
    SB;
    REDD(3, 1, 0, 2);
    ADD1(hv[1][1][3], ev[3]);
    SB;
    MF(C[2][0][1], WF[8], A[1][4][1]);
    SB;
    REDD(3, 1, 0, 3);
    SB;
    MF(C[2][1][1], WF[8], SR[4]);
    SB;
    MF(C[3][0][1], WF[8], A[1][5][1]);
    SB;
    MF(C[3][1][1], WF[8], SR[5]);
    SB;
    Hn[0][0] = (f32x4){hv[0][0][0], hv[0][0][1], hv[0][0][2], hv[0][0][3]};
    Hn[1][0] = (f32x4){hv[1][0][0], hv[1][0][1], hv[1][0][2], hv[1][0][3]};
    Hn[0][1] = (f32x4){hv[0][1][0], hv[0][1][1], hv[0][1][2], hv[0][1][3]};
    Hn[1][1] = (f32x4){hv[1][1][0], hv[1][1][1], hv[1][1][2], hv[1][1][3]};
    MFV(Hn[0][0], WF[0], SL[0]);
    SB;
    MFV(Hn[1][0], WF[0], A[1][0][0]);
    SB;
    MFV(Hn[0][1], WF[0], SL[1]);
    SB;
    MFV(Hn[1][1], WF[0], A[1][1][0]);
    SB;
    REDD(2, 0, 1, 0);
    SB;
    MFV(Hn[0][0], WF[1], A[1][0][0]);
    SB;
    REDD(2, 0, 1, 1);
    SB;
    MFV(Hn[1][0], WF[1], A[1][0][1]);
    SB;
    REDD(2, 0, 1, 2);
    SB;
    MFV(Hn[0][1], WF[1], A[1][1][0]);
    SB;
    REDD(2, 0, 1, 3);
    SB;
    MFV(Hn[1][1], WF[1], A[1][1][1]);
    SB;
    REDD(2, 1, 1, 0);
    SB;
    MFV(Hn[0][0], WF[2], A[1][0][1]);
    SB;
    REDD(2, 1, 1, 1);
    SB;
    MFV(Hn[1][0], WF[2], SR[0]);
    SB;
    REDD(2, 1, 1, 2);
    SB;
    MFV(Hn[0][1], WF[2], A[1][1][1]);
    SB;
    REDD(2, 1, 1, 3);
    SB;
    MFV(Hn[1][1], WF[2], SR[1]);
    SB;
    REDD(3, 0, 1, 0);
    SB;
    MFV(Hn[0][0], WF[3], SL[1]);
    SB;
    REDD(3, 0, 1, 1);
    SB;
    MFV(Hn[1][0], WF[3], A[1][1][0]);
    SB;
    REDD(3, 0, 1, 2);
    SB;
    MFV(Hn[0][1], WF[3], SL[2]);
    SB;
    REDD(3, 0, 1, 3);
    SB;
    MFV(Hn[1][1], WF[3], A[1][2][0]);
    SB;
    REDD(3, 1, 1, 0);
    SB;
    MFV(Hn[0][0], WF[4], A[1][1][0]);
    SB;
    REDD(3, 1, 1, 1);
    SB;
    MFV(Hn[1][0], WF[4], A[1][1][1]);
    SB;
    REDD(3, 1, 1, 2);
    SB;
    MFV(Hn[0][1], WF[4], A[1][2][0]);
    SB;
    REDD(3, 1, 1, 3);
    SB;
    MFV(Hn[1][1], WF[4], A[1][2][1]);
    SB;
    BARRIER();
    RDP(0, 1);
    RDP(0, 2);
    SB;
    MFV(Hn[0][0], WF[5], A[1][1][1]);
    SB;
    RDP(0, 3);
    RDP(1, 1);
    SB;
    MFV(Hn[1][0], WF[5], SR[1]);
    SB;
    RDP(1, 2);
    RDP(1, 3);
    SB;
    MFV(Hn[0][1], WF[5], A[1][2][1]);
    SB;
    RDP(2, 1);
    RDP(2, 2);
    SB;
    MFV(Hn[1][1], WF[5], SR[2]);
    SB;
    RDP(2, 3);
    RDP(3, 1);
    SB;
    MFV(Hn[0][0], WF[6], SL[2]);
    SB;
    RDP(3, 2);
    RDP(3, 3);
    SB;
    MFV(Hn[1][0], WF[6], A[1][2][0]);
    SB;
    RDP(4, 1);
    RDP(4, 2);
    SB;
    MFV(Hn[0][1], WF[6], SL[3]);
    SB;
    RDP(4, 3);
    WT3(12, 0);
    RDP(5, 1);
    SB;
    MFV(Hn[1][1], WF[6], A[1][3][0]);
    SB;
    RDP(5, 2);
    RDP(5, 3);
    SB;
    MFV(Hn[0][0], WF[7], A[1][2][0]);
    SB;
    WT3(12, 1);
    RDP(6, 1);
    RDP(6, 2);
    SB;
    MFV(Hn[1][0], WF[7], A[1][2][1]);
    SB;
    RDP(6, 3);
    WT3(12, 2);
    RDP(7, 1);
    SB;
    MFV(Hn[0][1], WF[7], A[1][3][0]);
    SB;
    RDP(7, 2);
    RDP(7, 3);
    SB;
    MFV(Hn[1][1], WF[7], A[1][3][1]);
    SB;
    ADD2(P[0][0], P[0][2]);
    SB;
    MFV(Hn[0][0], WF[8], A[1][2][1]);
    SB;
    ADD2(P[0][0], P[0][1]);
    SB;
    MFV(Hn[1][0], WF[8], SR[2]);
    SB;
    ADD2(P[1][0], P[1][2]);
    SB;
    MFV(Hn[0][1], WF[8], A[1][3][1]);
    SB;
    ADD2(P[1][0], P[1][1]);
    SB;
    MFV(Hn[1][1], WF[8], SR[3]);
    SB;
    ADD2(P[2][0], P[2][2]);
    SB;
    P[2][0] += P[2][1];
    WT3(12, 3);
    P[3][0] = (P[3][0] + P[3][1]) + P[3][2];
    WT3(9, 4);
    P[4][0] = (P[4][0] + P[4][1]) + P[4][2];
    WT3(6, 5);
    P[5][0] = (P[5][0] + P[5][1]) + P[5][2];
    WT3(3, 6);
    P[6][0] = (P[6][0] + P[6][1]) + P[6][2];
    WT3(0, 7);
    P[7][0] = (P[7][0] + P[7][1]) + P[7][2];
    SB;
    asm volatile("s_nop 1" : "+v"(Hn[0][0]), "+v"(Hn[1][0]));
    FIN(0); FIN(2); SB;
    asm volatile("s_nop 3" : "+v"(Hn[0][1]), "+v"(Hn[1][1]));
    FIN(4); FIN(6);
    }
    if (wpb != 0) {
    MF(C[0][0][1], WF[0], SL[0]);
    SB;
    ACCMV(hv[0][0][0], C[2][0][1][0]);
    SB;
    MF(C[0][1][1], WF[0], A[1][0][0]);
    SB;
    REDD(0, 0, 0, 0);
    ADD1(hv[0][0][0], ev[0]);
    SB;
    MF(C[1][0][1], WF[0], SL[1]);
    SB;
    REDD(0, 0, 0, 1);
    ACCMV(hv[0][0][1], C[2][0][1][1]);
    SB;
    MF(C[1][1][1], WF[0], A[1][1][0]);
    SB;
    REDD(0, 0, 0, 2);
    ADD1(hv[0][0][1], ev[1]);
    SB;
    MF(C[0][0][1], WF[1], A[1][0][0]);
    SB;
    REDD(0, 0, 0, 3);
    ACCMV(hv[0][0][2], C[2][0][1][2]);
    SB;
    MF(C[0][1][1], WF[1], A[1][0][1]);
    SB;
    REDD(0, 1, 0, 0);
    ADD1(hv[0][0][2], ev[2]);
    SB;
    MF(C[1][0][1], WF[1], A[1][1][0]);
    SB;
    REDD(0, 1, 0, 1);
    ACCMV(hv[0][0][3], C[2][0][1][3]);
    SB;
    MF(C[1][1][1], WF[1], A[1][1][1]);
    SB;
    REDD(0, 1, 0, 2);
    ADD1(hv[0][0][3], ev[3]);
    SB;
    MF(C[0][0][1], WF[2], A[1][0][1]);
    SB;
    REDD(0, 1, 0, 3);
    ACCMV(hv[1][0][0], C[2][1][1][0]);
    SB;
    MF(C[0][1][1], WF[2], SR[0]);
    SB;
    REDD(1, 0, 0, 0);
    ADD1(hv[1][0][0], ev[0]);
    SB;
    MF(C[1][0][1], WF[2], A[1][1][1]);
    SB;
    REDD(1, 0, 0, 1);
    ACCMV(hv[1][0][1], C[2][1][1][1]);
    SB;
    MF(C[1][1][1], WF[2], SR[1]);
    SB;
    REDD(1, 0, 0, 2);
    ADD1(hv[1][0][1], ev[1]);
    SB;
    MF(C[0][0][1], WF[3], SL[1]);
    SB;
    REDD(1, 0, 0, 3);
    ACCMV(hv[1][0][2], C[2][1][1][2]);
    SB;
    MF(C[0][1][1], WF[3], A[1][1][0]);
    SB;
    REDD(1, 1, 0, 0);
    ADD1(hv[1][0][2], ev[2]);
    SB;
    MF(C[1][0][1], WF[3], SL[2]);
    SB;
    REDD(1, 1, 0, 1);
    ACCMV(hv[1][0][3], C[2][1][1][3]);
    SB;
    MF(C[1][1][1], WF[3], A[1][2][0]);
    SB;
    REDD(1, 1, 0, 2);
    ADD1(hv[1][0][3], ev[3]);
    SB;
    MF(C[0][0][1], WF[4], A[1][1][0]);
    SB;
    REDD(1, 1, 0, 3);
    ACCMV(hv[0][1][0], C[3][0][1][0]);
    SB;
    MF(C[0][1][1], WF[4], A[1][1][1]);
    SB;
    REDD(2, 0, 0, 0);
    ADD1(hv[0][1][0], ev[0]);
    SB;
    MF(C[1][0][1], WF[4], A[1][2][0]);
    SB;
    REDD(2, 0, 0, 1);
    ACCMV(hv[0][1][1], C[3][0][1][1]);
    SB;
    MF(C[1][1][1], WF[4], A[1][2][1]);
    SB;
    REDD(2, 0, 0, 2);
    ADD1(hv[0][1][1], ev[1]);
    SB;
    MF(C[0][0][1], WF[5], A[1][1][1]);
    SB;
    REDD(2, 0, 0, 3);
    ACCMV(hv[0][1][2], C[3][0][1][2]);
    SB;
    MF(C[0][1][1], WF[5], SR[1]);
    SB;
    REDD(2, 1, 0, 0);
    ADD1(hv[0][1][2], ev[2]);
    SB;
    MF(C[1][0][1], WF[5], A[1][2][1]);
    SB;
    REDD(2, 1, 0, 1);
    ACCMV(hv[0][1][3], C[3][0][1][3]);
    SB;
    MF(C[1][1][1], WF[5], SR[2]);
    SB;
    REDD(2, 1, 0, 2);
    ADD1(hv[0][1][3], ev[3]);
    SB;
    MF(C[0][0][1], WF[6], SL[2]);
    SB;
    REDD(2, 1, 0, 3);
    ACCMV(hv[1][1][0], C[3][1][1][0]);
    SB;
    MF(C[0][1][1], WF[6], A[1][2][0]);
    SB;
    REDD(3, 0, 0, 0);
    ADD1(hv[1][1][0], ev[0]);
    SB;
    MF(C[1][0][1], WF[6], SL[3]);
    SB;
    REDD(3, 0, 0, 1);
    ACCMV(hv[1][1][1], C[3][1][1][1]);
    SB;
    MF(C[1][1][1], WF[6], A[1][3][0]);
    SB;
    REDD(3, 0, 0, 2);
    ADD1(hv[1][1][1], ev[1]);
    SB;
    MF(C[0][0][1], WF[7], A[1][2][0]);
    SB;
    REDD(3, 0, 0, 3);
    ACCMV(hv[1][1][2], C[3][1][1][2]);
    SB;
    MF(C[0][1][1], WF[7], A[1][2][1]);
    SB;
    REDD(3, 1, 0, 0);
    ADD1(hv[1][1][2], ev[2]);
    SB;
    MF(C[1][0][1], WF[7], A[1][3][0]);
    SB;
    REDD(3, 1, 0, 1);
    ACCMV(hv[1][1][3], C[3][1][1][3]);
    SB;
    MF(C[1][1][1], WF[7], A[1][3][1]);
    SB;
    REDD(3, 1, 0, 2);
    ADD1(hv[1][1][3], ev[3]);
    SB;
    MF(C[0][0][1], WF[8], A[1][2][1]);
    SB;
    REDD(3, 1, 0, 3);
    SB;
    MF(C[0][1][1], WF[8], SR[2]);
    SB;
    MF(C[1][0][1], WF[8], A[1][3][1]);
    SB;
    MF(C[1][1][1], WF[8], SR[3]);
    SB;
    Hn[0][0] = (f32x4){hv[0][0][0], hv[0][0][1], hv[0][0][2], hv[0][0][3]};
    Hn[1][0] = (f32x4){hv[1][0][0], hv[1][0][1], hv[1][0][2], hv[1][0][3]};
    Hn[0][1] = (f32x4){hv[0][1][0], hv[0][1][1], hv[0][1][2], hv[0][1][3]};
    Hn[1][1] = (f32x4){hv[1][1][0], hv[1][1][1], hv[1][1][2], hv[1][1][3]};
    MFV(Hn[0][0], WF[0], SL[2]);
    SB;
    MFV(Hn[1][0], WF[0], A[1][2][0]);
    SB;
    MFV(Hn[0][1], WF[0], SL[3]);
    SB;
    MFV(Hn[1][1], WF[0], A[1][3][0]);
    SB;
    REDD(0, 0, 1, 0);
    SB;
    MFV(Hn[0][0], WF[1], A[1][2][0]);
    SB;
    REDD(0, 0, 1, 1);
    SB;
    MFV(Hn[1][0], WF[1], A[1][2][1]);
    SB;
    REDD(0, 0, 1, 2);
    SB;
    MFV(Hn[0][1], WF[1], A[1][3][0]);
    SB;
    REDD(0, 0, 1, 3);
    SB;
    MFV(Hn[1][1], WF[1], A[1][3][1]);
    SB;
    REDD(0, 1, 1, 0);
    SB;
    MFV(Hn[0][0], WF[2], A[1][2][1]);
    SB;
    REDD(0, 1, 1, 1);
    SB;
    MFV(Hn[1][0], WF[2], SR[2]);
    SB;
    REDD(0, 1, 1, 2);
    SB;
    MFV(Hn[0][1], WF[2], A[1][3][1]);
    SB;
    REDD(0, 1, 1, 3);
    SB;
    MFV(Hn[1][1], WF[2], SR[3]);
    SB;
    REDD(1, 0, 1, 0);
    SB;
    MFV(Hn[0][0], WF[3], SL[3]);
    SB;
    REDD(1, 0, 1, 1);
    SB;
    MFV(Hn[1][0], WF[3], A[1][3][0]);
    SB;
    REDD(1, 0, 1, 2);
    SB;
    MFV(Hn[0][1], WF[3], SL[4]);
    SB;
    REDD(1, 0, 1, 3);
    SB;
    MFV(Hn[1][1], WF[3], A[1][4][0]);
    SB;
    REDD(1, 1, 1, 0);
    SB;
    MFV(Hn[0][0], WF[4], A[1][3][0]);
    SB;
    REDD(1, 1, 1, 1);
    SB;
    MFV(Hn[1][0], WF[4], A[1][3][1]);
    SB;
    REDD(1, 1, 1, 2);
    SB;
    MFV(Hn[0][1], WF[4], A[1][4][0]);
    SB;
    REDD(1, 1, 1, 3);
    SB;
    MFV(Hn[1][1], WF[4], A[1][4][1]);
    SB;
    BARRIER();
    RDP(0, 1);
    RDP(0, 2);
    SB;
    MFV(Hn[0][0], WF[5], A[1][3][1]);
    SB;
    RDP(0, 3);
    RDP(1, 1);
    SB;
    MFV(Hn[1][0], WF[5], SR[3]);
    SB;
    RDP(1, 2);
    RDP(1, 3);
    SB;
    MFV(Hn[0][1], WF[5], A[1][4][1]);
    SB;
    RDP(2, 1);
    RDP(2, 2);
    SB;
    MFV(Hn[1][1], WF[5], SR[4]);
    SB;
    RDP(2, 3);
    RDP(3, 1);
    SB;
    MFV(Hn[0][0], WF[6], SL[4]);
    SB;
    RDP(3, 2);
    RDP(3, 3);
    SB;
    MFV(Hn[1][0], WF[6], A[1][4][0]);
    SB;
    RDP(4, 1);
    RDP(4, 2);
    SB;
    MFV(Hn[0][1], WF[6], SL[5]);
    SB;
    RDP(4, 3);
    WT3(12, 0);
    RDP(5, 1);
    SB;
    MFV(Hn[1][1], WF[6], A[1][5][0]);
    SB;
    RDP(5, 2);
    RDP(5, 3);
    SB;
    MFV(Hn[0][0], WF[7], A[1][4][0]);
    SB;
    WT3(12, 1);
    RDP(6, 1);
    RDP(6, 2);
    SB;
    MFV(Hn[1][0], WF[7], A[1][4][1]);
    SB;
    RDP(6, 3);
    WT3(12, 2);
    RDP(7, 1);
    SB;
    MFV(Hn[0][1], WF[7], A[1][5][0]);
    SB;
    RDP(7, 2);
    RDP(7, 3);
    SB;
    MFV(Hn[1][1], WF[7], A[1][5][1]);
    SB;
    ADD2(P[0][0], P[0][2]);
    SB;
    MFV(Hn[0][0], WF[8], A[1][4][1]);
    SB;
    ADD2(P[0][0], P[0][1]);
    SB;
    MFV(Hn[1][0], WF[8], SR[4]);
    SB;
    ADD2(P[1][0], P[1][2]);
    SB;
    MFV(Hn[0][1], WF[8], A[1][5][1]);
    SB;
    ADD2(P[1][0], P[1][1]);
    SB;
    MFV(Hn[1][1], WF[8], SR[5]);
    SB;
    ADD2(P[2][0], P[2][2]);
    SB;
    P[2][0] += P[2][1];
    WT3(12, 3);
    P[3][0] = (P[3][0] + P[3][1]) + P[3][2];
    WT3(9, 4);
    P[4][0] = (P[4][0] + P[4][1]) + P[4][2];
    WT3(6, 5);
    P[5][0] = (P[5][0] + P[5][1]) + P[5][2];
    WT3(3, 6);
    P[6][0] = (P[6][0] + P[6][1]) + P[6][2];
    WT3(0, 7);
    P[7][0] = (P[7][0] + P[7][1]) + P[7][2];
    SB;
    asm volatile("s_nop 1" : "+v"(Hn[0][0]), "+v"(Hn[1][0]));
    FIN(0); FIN(2); SB;
    asm volatile("s_nop 3" : "+v"(Hn[0][1]), "+v"(Hn[1][1]));
    FIN(4); FIN(6);
    }
    }
    H[0][0] = Hn[0][0]; H[0][1] = Hn[0][1]; H[1][0] = Hn[1][0]; H[1][1] = Hn[1][1];
    if (cv == 0) stats_publish(1);
  }
  {
    u32 xr[2][4];
    XREAD("");
    const __amdgpu_buffer_rsrc_t srdO = __builtin_amdgcn_make_buffer_rsrc((void*)out, (short)0, IMG_BYTES, 0x00020000);
#pragma unroll
    for (int k = 0; k < 4; k++)
#pragma unroll
      for (int jj = 0; jj < 2; jj++)
        __builtin_amdgcn_raw_buffer_store_b32(cvtpk(H[0][jj][k] + bflo(xr[jj][k]), H[1][jj][k] + bfhi(xr[jj][k])), srdO, xoff + (k & 1) * 2048u + jj * 64u,
                                              (k >> 1) * 4096u, 16);
  }
  asm volatile("s_waitcnt vmcnt(0)" ::: "memory");
}


extern "C" void kernel_launch(void* const* d_in, const int* in_sizes, int n_in, void* d_out, int out_size, void* d_ws, size_t ws_size,
                              hipStream_t stream) {
  resblock_k<<<256, 256, 0, stream>>>((const u16*)d_in[0], (const u16*)d_in[1], (const u16*)d_in[2], (const u16*)d_in[3], (const u16*)d_in[4],
                                      (const u16*)d_in[5], (const u16*)d_in[6], (const u16*)d_in[7], (const u16*)d_in[8], (char*)d_ws, (u16*)d_out);
}
```
